```python
import torch

def _softmax(x, axis=-1):
    return torch.softmax(x, dim=axis)

def _gelu(x):
    return torch.nn.functional.gelu(x, approximate='tanh')

def _silu(x):
    return torch.nn.functional.silu(x)

def _sigmoid(x):
    return torch.sigmoid(x)

def _layernorm(x, axis=-1, eps=1e-05):
    m = x.mean(dim=axis, keepdim=True)
    v = x.var(dim=axis, keepdim=True, unbiased=False)
    return (x - m) / torch.sqrt(v + eps)

def _scatter_add(tgt, idx, src, axis=0):
    return tgt.clone().index_add_(axis, idx, src)

def _segment_sum(src, seg, n):
    out = torch.zeros((n,) + src.shape[1:], dtype=src.dtype)
    return out.index_add_(0, seg, src)

def _pad(x, pw):
    flat = tuple((v for pair in reversed(pw) for v in pair))
    return torch.nn.functional.pad(x, flat)
kernel_name = 'standalone__dit_block__diffusion'

def setup_inputs():

    _w = lambda a: (lambda t: t.to(torch.bfloat16) if t.is_floating_point() else t)(torch.as_tensor(a))
    B, N, D = (2, 1024, 768)
    F = 4 * D
    return {'x': _w((lambda *s: torch.randn(*s, dtype=torch.bfloat16) * 0.05)(B, N, D)), 'c': _w((lambda *s: torch.randn(*s, dtype=torch.bfloat16) * 0.2)(B, D)), 'wqkv': _w((lambda *s: torch.randn(*s, dtype=torch.bfloat16) * 0.05)(D, 3 * D)), 'wo': _w((lambda *s: torch.randn(*s, dtype=torch.bfloat16) * 0.05)(D, D)), 'w_mod': _w((lambda *s: torch.randn(*s, dtype=torch.bfloat16) * 0.2)(D, 6 * D)), 'w_fc1': _w((lambda *s: torch.randn(*s, dtype=torch.bfloat16) * 0.05)(D, F)), 'w_fc2': _w((lambda *s: torch.randn(*s, dtype=torch.bfloat16) * 0.05)(F, D))}

def reference(x, c, wqkv, wo, w_mod, w_fc1, w_fc2):
    B, N, D = x.shape
    H = 2
    Dh = D // H
    mod = (_silu(c) @ w_mod).reshape(B, 6, 1, D)
    shift_a, scale_a, gate_a, shift_m, scale_m, gate_m = [mod[:, i] for i in range(6)]
    h = _layernorm(x, axis=-1) * (1 + scale_a) + shift_a
    qkv = (h @ wqkv).reshape(B, N, 3, H, Dh)
    q, k, v = (qkv[:, :, 0].clone(), qkv[:, :, 1].clone(), qkv[:, :, 2].clone())
    attn = _softmax(torch.einsum('bqhd,bkhd->bhqk', q, k) * Dh ** (-0.5), axis=-1)
    a = torch.einsum('bhqk,bkhd->bqhd', attn, v).reshape(B, N, D) @ wo
    x = x + gate_a * a
    h2 = _layernorm(x, axis=-1) * (1 + scale_m) + shift_m
    m = _gelu(h2 @ w_fc1) @ w_fc2
    return x + gate_m * m
```

```cpp
#include <hip/hip_runtime.h>
#include <hip/hip_bf16.h>
#include <stdint.h>

typedef unsigned short u16;
typedef __attribute__((ext_vector_type(8))) __bf16 bf16x8;
typedef __attribute__((ext_vector_type(4))) __bf16 bf16x4;
typedef __attribute__((ext_vector_type(2))) __bf16 bf16x2;
typedef __attribute__((ext_vector_type(4))) float f32x4;
typedef __attribute__((ext_vector_type(2))) float f32x2;
typedef __attribute__((ext_vector_type(4))) unsigned u32x4;
typedef __attribute__((ext_vector_type(2))) unsigned u32x2;
#define LAS __attribute__((address_space(3)))
#define GAS __attribute__((address_space(1)))

constexpr int NB = 2, NSEQ = 1024, DM = 768, NH = 2, DH = 384, FF = 3072;
constexpr int MROWS = NB * NSEQ;

__device__ __forceinline__ float bf2f(u16 v) { return __uint_as_float(((unsigned)v) << 16); }
__device__ __forceinline__ float bflo(unsigned v) { return __uint_as_float(v << 16); }
__device__ __forceinline__ float bfhi(unsigned v) { return __uint_as_float(v & 0xffff0000u); }
__device__ __forceinline__ unsigned pack2(float a, float b) {
  f32x2 f = {a, b};
  bf16x2 r = __builtin_convertvector(f, bf16x2);
  return __builtin_bit_cast(unsigned, r);
}
__device__ __forceinline__ float rbf(float a) {
  return bflo(pack2(a, 0.f) & 0xffffu);
}

#define WAITV(n) asm volatile("s_waitcnt vmcnt(%0)" ::"i"(n) : "memory")
#define FENCE() asm volatile("" ::: "memory")
#define BARRIER()                        \
  do {                                   \
    FENCE();                             \
    __builtin_amdgcn_s_barrier();        \
    FENCE();                             \
  } while (0)

template <int BM, int BN, int WM, int WN, int NS, bool BKN>
struct GC {
  static constexpr int NW = WM * WN;
  static constexpr int WTM = BM / WM, WTN = BN / WN, MT = WTM / 16, NT = WTN / 16;
  static constexpr int A_BYTES = BM * 128, B_BYTES = BN * 128, STAGE = A_BYTES + B_BYTES;
  static constexpr int NA = BM / 8, NBI = BN / 8;
  static constexpr int LA = NA / NW;
  static constexpr int LB = (NBI + NW - 1) / NW;
  static constexpr int L = LA + LB;
  static_assert(NA % NW == 0, "A instr split");
  static_assert(NS >= 3 && NS <= 5, "stages");
};

template <int BM, int BN, int WM, int WN, int NS, bool BKN, int T, class Hook>
__device__ __forceinline__ void gemm_main(const u16* __restrict__ Ab, int lda, const u16* __restrict__ Bb, int ldb,
                                          char* smem, f32x4 (&acc)[BM / WM / 16][BN / WN / 16], Hook hook) {
  using C = GC<BM, BN, WM, WN, NS, BKN>;
  constexpr int MT = C::MT, NT = C::NT, L = C::L, LA = C::LA, LB = C::LB, NW = C::NW;
  const int tid = threadIdx.x, lane = tid & 63;
  const int wave = __builtin_amdgcn_readfirstlane(tid >> 6);
  const int wm = wave % WM, wn = wave / WM;

  unsigned goffA[LA], goffB[LB];
  unsigned ldstB[LB];
#pragma unroll
  for (int i = 0; i < LA; ++i) {
    int q = i * NW + wave;
    int row = q * 8 + (lane >> 3);
    int lc = (lane & 7) ^ (row & 7);
    goffA[i] = (unsigned)(row * lda + lc * 8) * 2u;
  }
#pragma unroll
  for (int i = 0; i < LB; ++i) {
    int q = i * NW + wave;
    if (q >= C::NBI) q -= NW;
    ldstB[i] = (unsigned)(C::A_BYTES + q * 1024);
    if (BKN) {
      int st = q * 4 + (lane >> 4);
      int kb = st / (BN / 16), nb = st % (BN / 16);
      int i16 = lane & 15, prow = i16 >> 1, half = i16 & 1;
      int lrow = prow ^ ((kb & 1) << 2);
      goffB[i] = (unsigned)((kb * 8 + lrow) * ldb + nb * 16 + half * 8) * 2u;
    } else {
      int row = q * 8 + (lane >> 3);
      int lc = (lane & 7) ^ (row & 7);
      goffB[i] = (unsigned)(row * ldb + lc * 8) * 2u;
    }
  }
  const char* Abase = (const char*)Ab;
  const char* Bbase = (const char*)Bb;
  const size_t bstep = BKN ? (size_t)64 * ldb * 2 : 128;

  auto issue = [&](int t) {
    const int slot = t % NS;
    char* sb = smem + slot * C::STAGE;
    const char* pa = Abase + (size_t)t * 128;
    const char* pb = Bbase + (size_t)t * bstep;
#pragma unroll
    for (int i = 0; i < LA; ++i)
      __builtin_amdgcn_global_load_lds((const GAS unsigned*)(pa + goffA[i]),
                                       (LAS unsigned*)(sb + (i * NW + wave) * 1024), 16, 0, 0);
#pragma unroll
    for (int i = 0; i < LB; ++i)
      __builtin_amdgcn_global_load_lds((const GAS unsigned*)(pb + goffB[i]), (LAS unsigned*)(sb + ldstB[i]), 16, 0,
                                       0);
  };

  const int g = lane >> 4, i16 = lane & 15;
  unsigned aoff[2];
#pragma unroll
  for (int s = 0; s < 2; ++s)
    aoff[s] = (unsigned)((wm * C::WTM + i16) * 128 + (((s * 4 + g) ^ (lane & 7)) << 4));
  unsigned boff[2];
  if (BKN) {
    const int q4 = i16 >> 2, p = i16 & 3;
#pragma unroll
    for (int h = 0; h < 2; ++h)
      boff[h] = (unsigned)(C::A_BYTES + (g * (BN / 16) + wn * NT) * 256 + (((h * 4 + q4) ^ ((g & 1) << 2)) * 32) +
                           p * 8);
  } else {
#pragma unroll
    for (int s = 0; s < 2; ++s)
      boff[s] = (unsigned)(C::A_BYTES + (wn * C::WTN + i16) * 128 + (((s * 4 + g) ^ (lane & 7)) << 4));
  }

  auto rd_frag = [&](int slot, int s, bf16x8 (&fa)[MT], bf16x8 (&fb)[NT]) {
    const char* sb = smem + slot * C::STAGE;
#pragma unroll
    for (int n = 0; n < NT; ++n) {
      if (BKN) {
        bf16x4 lo = __builtin_amdgcn_ds_read_tr16_b64_v4bf16(
            (LAS bf16x4*)(sb + boff[0] + s * 4 * (BN / 16) * 256 + n * 256));
        bf16x4 hi = __builtin_amdgcn_ds_read_tr16_b64_v4bf16(
            (LAS bf16x4*)(sb + boff[1] + s * 4 * (BN / 16) * 256 + n * 256));
        fb[n] = __builtin_shufflevector(lo, hi, 0, 1, 2, 3, 4, 5, 6, 7);
      } else {
        fb[n] = *(const bf16x8*)(sb + boff[s] + n * 2048);
      }
    }
#pragma unroll
    for (int m = 0; m < MT; ++m) fa[m] = *(const bf16x8*)(sb + aoff[s] + m * 2048);
  };
  auto mma = [&](bf16x8 (&fa)[MT], bf16x8 (&fb)[NT]) {
#pragma unroll
    for (int m = 0; m < MT; ++m)
#pragma unroll
      for (int n = 0; n < NT; ++n)
        acc[m][n] = __builtin_amdgcn_mfma_f32_16x16x32_bf16(fb[n], fa[m], acc[m][n], 0, 0, 0);
  };

#pragma unroll
  for (int s = 0; s < NS - 1; ++s)
    if (s < T) issue(s);
  WAITV(L * (NS - 2));
  BARRIER();
  bf16x8 fa0[MT], fb0[NT], fa1[MT], fb1[NT];
  rd_frag(0, 0, fa0, fb0);
#pragma unroll 1
  for (int t = 0; t < T; ++t) {
    const int slot = t % NS;
    rd_frag(slot, 1, fa1, fb1);
    mma(fa0, fb0);
    if (t + 1 < T) {
      const int rem = T - 2 - t;
      if (NS == 5) {
        if (rem >= 2) WAITV(2 * L);
        else if (rem == 1) WAITV(L);
        else WAITV(0);
      } else if (NS == 4) {
        if (rem >= 1) WAITV(L);
        else WAITV(0);
      } else {
        WAITV(0);
      }
      BARRIER();
      if (t + NS - 1 < T) issue(t + NS - 1);
      rd_frag((t + 1) % NS, 0, fa0, fb0);
    }
    mma(fa1, fb1);
    hook(t);
  }
}

struct NoHook {
  __device__ __forceinline__ void operator()(int) const {}
};

template <int BM, int BN, int WM, int WN>
__device__ __forceinline__ void stage_acc(char* smem, f32x4 (&acc)[BM / WM / 16][BN / WN / 16]) {
  constexpr int MT = BM / WM / 16, NT = BN / WN / 16, RS = BN * 2 + 16;
  const int tid = threadIdx.x, lane = tid & 63, wave = tid >> 6;
  const int wm = wave % WM, wn = wave / WM;
  BARRIER();
#pragma unroll
  for (int m = 0; m < MT; ++m)
#pragma unroll
    for (int n = 0; n < NT; ++n) {
      int row = wm * (BM / WM) + m * 16 + (lane & 15);
      int col = wn * (BN / WN) + n * 16 + (lane >> 4) * 4;
      u32x2 v = {pack2(acc[m][n][0], acc[m][n][1]), pack2(acc[m][n][2], acc[m][n][3])};
      *(u32x2*)(smem + row * RS + col * 2) = v;
    }
  __syncthreads();
}

__global__ __launch_bounds__(256) void k_mod(const u16* __restrict__ c, const u16* __restrict__ wmod,
                                             float* __restrict__ mod) {
  __shared__ float red[2][64][33];
  const int tid = threadIdx.x, cg = tid & 3, kq = tid >> 2;
  const int col0 = blockIdx.x * 32 + cg * 8;
  u32x4 w[12];
#pragma unroll
  for (int i = 0; i < 12; ++i) {
    int k = i * 64 + kq;
    w[i] = *(const u32x4*)(wmod + (size_t)k * (6 * DM) + col0);
  }
  float a0[8], a1[8];
#pragma unroll
  for (int j = 0; j < 8; ++j) a0[j] = a1[j] = 0.f;
#pragma unroll
  for (int i = 0; i < 12; ++i) {
    int k = i * 64 + kq;
    float c0 = bf2f(c[k]), c1 = bf2f(c[DM + k]);
    float s0 = rbf(c0 / (1.f + __expf(-c0)));
    float s1 = rbf(c1 / (1.f + __expf(-c1)));
#pragma unroll
    for (int j = 0; j < 4; ++j) {
      float wl = bflo(w[i][j]), wh = bfhi(w[i][j]);
      a0[2 * j] += s0 * wl; a0[2 * j + 1] += s0 * wh;
      a1[2 * j] += s1 * wl; a1[2 * j + 1] += s1 * wh;
    }
  }
#pragma unroll
  for (int j = 0; j < 8; ++j) {
    red[0][kq][cg * 8 + j] = a0[j];
    red[1][kq][cg * 8 + j] = a1[j];
  }
  __syncthreads();
  if (tid < 64) {
    int b = tid >> 5, col = tid & 31;
    float s = 0.f;
    for (int q = 0; q < 64; ++q) s += red[b][q][col];
    mod[b * (6 * DM) + blockIdx.x * 32 + col] = rbf(s);
  }
}

__global__ __launch_bounds__(256) void k_lnmod(const u16* __restrict__ x, const float* __restrict__ mod,
                                               int shift_idx, u16* __restrict__ out) {
  const int lane = threadIdx.x & 63, row = blockIdx.x * 4 + (threadIdx.x >> 6);
  const int b = row >> 10;
  const u16* xr = x + (size_t)row * DM;
  float v[12];
#pragma unroll
  for (int i = 0; i < 3; ++i) {
    u32x2 u = *(const u32x2*)(xr + i * 256 + lane * 4);
    v[i * 4 + 0] = bflo(u[0]); v[i * 4 + 1] = bfhi(u[0]);
    v[i * 4 + 2] = bflo(u[1]); v[i * 4 + 3] = bfhi(u[1]);
  }
  float s = 0.f;
#pragma unroll
  for (int i = 0; i < 12; ++i) s += v[i];
#pragma unroll
  for (int o = 32; o; o >>= 1) s += __shfl_xor(s, o);
  const float mean = s * (1.f / DM);
  float q = 0.f;
#pragma unroll
  for (int i = 0; i < 12; ++i) { float d = v[i] - mean; q += d * d; }
#pragma unroll
  for (int o = 32; o; o >>= 1) q += __shfl_xor(q, o);
  const float mb = rbf(mean), vb = rbf(q * (1.f / DM));
  const float sd = rbf(__fsqrt_rn(rbf(vb + 1e-5f)));
  const float* shift = mod + b * (6 * DM) + shift_idx * DM;
  const float* scale = shift + DM;
#pragma unroll
  for (int i = 0; i < 3; ++i) {
    int c0 = i * 256 + lane * 4;
    f32x4 sc = *(const f32x4*)(scale + c0), sh = *(const f32x4*)(shift + c0);
    float h[4];
#pragma unroll
    for (int j = 0; j < 4; ++j) {
      float n = rbf(rbf(v[i * 4 + j] - mb) / sd);
      h[j] = rbf(n * rbf(1.f + sc[j])) + sh[j];
    }
    u32x2 o = {pack2(h[0], h[1]), pack2(h[2], h[3])};
    *(u32x2*)(out + (size_t)row * DM + c0) = o;
  }
}

__global__ __launch_bounds__(256) void k_qkv(const u16* __restrict__ h, const u16* __restrict__ w,
                                             u16* __restrict__ qkv) {
  extern __shared__ __attribute__((aligned(16))) char smem[];
  constexpr int BM = 128, BN = 144, WM = 4, WN = 1, NS = 4;
  const int x = blockIdx.x & 7, j = blockIdx.x >> 3;
  const int tm = (x >> 2) * 8 + (j >> 2), tn = (x & 3) * 4 + (j & 3);
  const int m0 = tm * BM, n0 = tn * BN;
  f32x4 acc[2][9] = {};
  gemm_main<BM, BN, WM, WN, NS, true, 12>(h + (size_t)m0 * DM, DM, w + n0, 3 * DM, smem, acc, NoHook());
  stage_acc<BM, BN, WM, WN>(smem, acc);
  constexpr int RS = BN * 2 + 16, CPR = BN / 8;
  for (int c = threadIdx.x; c < BM * CPR; c += 256) {
    int row = c / CPR, c8 = c % CPR;
    u32x4 v = *(const u32x4*)(smem + row * RS + c8 * 16);
    *(u32x4*)(qkv + (size_t)(m0 + row) * (3 * DM) + n0 + c8 * 8) = v;
  }
}

__global__ __launch_bounds__(256) void k_scores(const u16* __restrict__ qkv, u16* __restrict__ E,
                                                f32x2* __restrict__ st) {
  extern __shared__ __attribute__((aligned(16))) char smem[];
  constexpr int BM = 128, BN = 128, WM = 2, WN = 2, NS = 4;
  const int x = blockIdx.x & 7, j = blockIdx.x >> 3;
  const int bh = x >> 1, b = bh >> 1, hh = bh & 1;
  const int tm = (x & 1) * 4 + (j >> 3), tn = j & 7;
  const int m0 = tm * BM, n0 = tn * BN;
  const u16* Q = qkv + (size_t)(b * NSEQ + m0) * (3 * DM) + hh * DH;
  const u16* K = qkv + (size_t)(b * NSEQ + n0) * (3 * DM) + DM + hh * DH;
  f32x4 acc[4][4] = {};
  gemm_main<BM, BN, WM, WN, NS, false, 6>(Q, 3 * DM, K, 3 * DM, smem, acc, NoHook());
  stage_acc<BM, BN, WM, WN>(smem, acc);
  constexpr int RS = BN * 2 + 16;
  const float scale = 0.05103103630798288f;
  const int l16 = threadIdx.x & 15, r0 = threadIdx.x >> 4;
#pragma unroll 2
  for (int i = 0; i < 8; ++i) {
    int row = r0 + i * 16;
    u32x4 v = *(const u32x4*)(smem + row * RS + l16 * 16);
    float s[8];
#pragma unroll
    for (int e = 0; e < 4; ++e) {
      s[2 * e] = rbf(bflo(v[e]) * scale);
      s[2 * e + 1] = rbf(bfhi(v[e]) * scale);
    }
    float mx = s[0];
#pragma unroll
    for (int e = 1; e < 8; ++e) mx = fmaxf(mx, s[e]);
#pragma unroll
    for (int o = 8; o; o >>= 1) mx = fmaxf(mx, __shfl_xor(mx, o));
    float sum = 0.f;
#pragma unroll
    for (int e = 0; e < 8; ++e) { s[e] = __expf(s[e] - mx); sum += s[e]; }
#pragma unroll
    for (int o = 8; o; o >>= 1) sum += __shfl_xor(sum, o);
    u32x4 o4 = {pack2(s[0], s[1]), pack2(s[2], s[3]), pack2(s[4], s[5]), pack2(s[6], s[7])};
    *(u32x4*)(E + ((size_t)bh * NSEQ + m0 + row) * NSEQ + n0 + l16 * 8) = o4;
    if (l16 == 0) st[((size_t)bh * NSEQ + m0 + row) * 8 + tn] = f32x2{mx, sum};
  }
}

__global__ __launch_bounds__(256) void k_pv(const u16* __restrict__ E, const f32x2* __restrict__ st,
                                            const u16* __restrict__ qkv, u16* __restrict__ O) {
  extern __shared__ __attribute__((aligned(16))) char smem[];
  constexpr int BM = 64, BN = 96, WM = 2, WN = 2, NS = 5;
  using C = GC<BM, BN, WM, WN, NS, true>;
  const int x = blockIdx.x & 7, j = blockIdx.x >> 3;
  const int bh = x >> 1, b = bh >> 1, hh = bh & 1;
  const int tm = (x & 1) * 8 + (j >> 2), tn = j & 3;
  const int m0 = tm * BM, n0 = tn * BN;
  float* ftab = (float*)(smem + NS * C::STAGE);
  {
    const int tid = threadIdx.x;
    if (tid < 64) {
      const f32x2* p = st + ((size_t)bh * NSEQ + m0 + tid) * 8;
      f32x2 v[8];
#pragma unroll
      for (int t = 0; t < 8; ++t) v[t] = p[t];
      float M = v[0][0];
#pragma unroll
      for (int t = 1; t < 8; ++t) M = fmaxf(M, v[t][0]);
      float e[8], Ls = 0.f;
#pragma unroll
      for (int t = 0; t < 8; ++t) { e[t] = __expf(v[t][0] - M); Ls += e[t] * v[t][1]; }
      float inv = 1.f / Ls;
#pragma unroll
      for (int t = 0; t < 8; ++t) ftab[tid * 8 + t] = e[t] * inv;
    }
    __syncthreads();
  }
  const u16* A = E + ((size_t)bh * NSEQ + m0) * NSEQ;
  const u16* V = qkv + (size_t)(b * NSEQ) * (3 * DM) + 2 * DM + hh * DH + n0;
  f32x4 acc[2][3] = {};
  f32x4 tot[2][3] = {};
  const int lane = threadIdx.x & 63, wave = threadIdx.x >> 6, wm = wave % WM;
  auto hook = [&](int t) {
    if (t & 1) {
      const int ch = t >> 1;
#pragma unroll
      for (int m = 0; m < 2; ++m) {
        float f = ftab[(wm * 32 + m * 16 + (lane & 15)) * 8 + ch];
#pragma unroll
        for (int n = 0; n < 3; ++n) {
          tot[m][n] += acc[m][n] * f;
          acc[m][n] = f32x4{0.f, 0.f, 0.f, 0.f};
        }
      }
    }
  };
  gemm_main<BM, BN, WM, WN, NS, true, 16>(A, NSEQ, V, 3 * DM, smem, acc, hook);
  stage_acc<BM, BN, WM, WN>(smem, tot);
  constexpr int RS = BN * 2 + 16, CPR = BN / 8;
  for (int c = threadIdx.x; c < BM * CPR; c += 256) {
    int row = c / CPR, c8 = c % CPR;
    u32x4 v = *(const u32x4*)(smem + row * RS + c8 * 16);
    *(u32x4*)(O + (size_t)(b * NSEQ + m0 + row) * DM + hh * DH + n0 + c8 * 8) = v;
  }
}

__global__ __launch_bounds__(256) void k_wo(const u16* __restrict__ O, const u16* __restrict__ wo,
                                            const u16* __restrict__ xin, const float* __restrict__ mod,
                                            u16* __restrict__ x1) {
  extern __shared__ __attribute__((aligned(16))) char smem[];
  constexpr int BM = 64, BN = 96, WM = 2, WN = 2, NS = 5;
  const int x = blockIdx.x & 7, j = blockIdx.x >> 3;
  const int tm = (x >> 1) * 8 + (j >> 2), tn = (x & 1) * 4 + (j & 3);
  const int m0 = tm * BM, n0 = tn * BN, b = m0 >> 10;
  f32x4 acc[2][3] = {};
  gemm_main<BM, BN, WM, WN, NS, true, 12>(O + (size_t)m0 * DM, DM, wo + n0, DM, smem, acc, NoHook());
  stage_acc<BM, BN, WM, WN>(smem, acc);
  constexpr int RS = BN * 2 + 16, CPR = BN / 8;
  const float* gate = mod + b * (6 * DM) + 2 * DM + n0;
  for (int c = threadIdx.x; c < BM * CPR; c += 256) {
    int row = c / CPR, c8 = c % CPR;
    u32x4 v = *(const u32x4*)(smem + row * RS + c8 * 16);
    u32x4 xv = *(const u32x4*)(xin + (size_t)(m0 + row) * DM + n0 + c8 * 8);
    f32x4 g0 = *(const f32x4*)(gate + c8 * 8), g1 = *(const f32x4*)(gate + c8 * 8 + 4);
    u32x4 o;
#pragma unroll
    for (int e = 0; e < 4; ++e) {
      float ga = e < 2 ? g0[2 * e] : g1[2 * e - 4], gb = e < 2 ? g0[2 * e + 1] : g1[2 * e - 3];
      float r0 = bflo(xv[e]) + rbf(ga * bflo(v[e]));
      float r1 = bfhi(xv[e]) + rbf(gb * bfhi(v[e]));
      o[e] = pack2(r0, r1);
    }
    *(u32x4*)(x1 + (size_t)(m0 + row) * DM + n0 + c8 * 8) = o;
  }
}

__device__ __forceinline__ float gelu_tanh(float u) {
  float z = 0.7978845608028654f * (u + 0.044715f * u * u * u);
  return u / (1.f + __expf(-2.f * z));
}
__global__ __launch_bounds__(256) void k_fc1(const u16* __restrict__ h2, const u16* __restrict__ w1,
                                             u16* __restrict__ f) {
  extern __shared__ __attribute__((aligned(16))) char smem[];
  constexpr int BM = 128, BN = 192, WM = 2, WN = 2, NS = 4;
  const int x = blockIdx.x & 7, j = blockIdx.x >> 3;
  const int tm = (x >> 2) * 8 + (j >> 2), tn = (x & 3) * 4 + (j & 3);
  const int m0 = tm * BM, n0 = tn * BN;
  f32x4 acc[4][6] = {};
  gemm_main<BM, BN, WM, WN, NS, true, 12>(h2 + (size_t)m0 * DM, DM, w1 + n0, FF, smem, acc, NoHook());
  stage_acc<BM, BN, WM, WN>(smem, acc);
  constexpr int RS = BN * 2 + 16, CPR = BN / 8;
  for (int c = threadIdx.x; c < BM * CPR; c += 256) {
    int row = c / CPR, c8 = c % CPR;
    u32x4 v = *(const u32x4*)(smem + row * RS + c8 * 16);
    u32x4 o;
#pragma unroll
    for (int e = 0; e < 4; ++e) o[e] = pack2(gelu_tanh(bflo(v[e])), gelu_tanh(bfhi(v[e])));
    *(u32x4*)(f + (size_t)(m0 + row) * FF + n0 + c8 * 8) = o;
  }
}

__global__ __launch_bounds__(256) void k_fc2(const u16* __restrict__ f, const u16* __restrict__ w2,
                                             float* __restrict__ part) {
  extern __shared__ __attribute__((aligned(16))) char smem[];
  constexpr int BM = 128, BN = 192, WM = 2, WN = 2, NS = 4;
  const int x = blockIdx.x & 7, j = blockIdx.x >> 3;
  const int ks = x >> 1, tm = (x & 1) * 8 + (j >> 2), tn = j & 3;
  const int m0 = tm * BM, n0 = tn * BN;
  f32x4 acc[4][6] = {};
  gemm_main<BM, BN, WM, WN, NS, true, 12>(f + (size_t)m0 * FF + ks * 768, FF, w2 + (size_t)ks * 768 * DM + n0, DM,
                                          smem, acc, NoHook());
  const int lane = threadIdx.x & 63, wave = threadIdx.x >> 6, wm = wave % WM, wn = wave / WM;
  float* P = part + (size_t)ks * MROWS * DM;
#pragma unroll
  for (int m = 0; m < 4; ++m)
#pragma unroll
    for (int n = 0; n < 6; ++n) {
      int row = m0 + wm * 64 + m * 16 + (lane & 15);
      int col = n0 + wn * 96 + n * 16 + (lane >> 4) * 4;
      *(f32x4*)(P + (size_t)row * DM + col) = acc[m][n];
    }
}

__global__ __launch_bounds__(256) void k_final(const float* __restrict__ part, const u16* __restrict__ x1,
                                               const float* __restrict__ mod, u16* __restrict__ out) {
  const int idx = blockIdx.x * 256 + threadIdx.x;
  const int row = idx / (DM / 8), c8 = idx % (DM / 8), b = row >> 10;
  const size_t off = (size_t)row * DM + c8 * 8;
  f32x4 s0 = {0.f, 0.f, 0.f, 0.f}, s1 = s0;
#pragma unroll
  for (int k = 0; k < 4; ++k) {
    s0 += *(const f32x4*)(part + (size_t)k * MROWS * DM + off);
    s1 += *(const f32x4*)(part + (size_t)k * MROWS * DM + off + 4);
  }
  const float* gate = mod + b * (6 * DM) + 5 * DM + c8 * 8;
  f32x4 g0 = *(const f32x4*)gate, g1 = *(const f32x4*)(gate + 4);
  u32x4 xv = *(const u32x4*)(x1 + off);
  u32x4 o;
#pragma unroll
  for (int e = 0; e < 4; ++e) {
    float ma = e < 2 ? s0[2 * e] : s1[2 * e - 4], mb = e < 2 ? s0[2 * e + 1] : s1[2 * e - 3];
    float ga = e < 2 ? g0[2 * e] : g1[2 * e - 4], gb = e < 2 ? g0[2 * e + 1] : g1[2 * e - 3];
    float r0 = bflo(xv[e]) + rbf(ga * rbf(ma));
    float r1 = bfhi(xv[e]) + rbf(gb * rbf(mb));
    o[e] = pack2(r0, r1);
  }
  *(u32x4*)(out + off) = o;
}

extern "C" void kernel_launch(void* const* d_in, const int* in_sizes, int n_in, void* d_out, int out_size,
                              void* d_ws, size_t ws_size, hipStream_t stream) {
  const u16* x = (const u16*)d_in[0];
  const u16* c = (const u16*)d_in[1];
  const u16* wqkv = (const u16*)d_in[2];
  const u16* wo = (const u16*)d_in[3];
  const u16* wmod = (const u16*)d_in[4];
  const u16* w1 = (const u16*)d_in[5];
  const u16* w2 = (const u16*)d_in[6];
  u16* out = (u16*)d_out;

  char* ws = (char*)d_ws;
  size_t o = 0;
  auto take = [&](size_t bytes) { char* p = ws + o; o += (bytes + 255) & ~(size_t)255; return p; };
  float* mod = (float*)take(2 * 6 * DM * 4);
  u16* h = (u16*)take((size_t)MROWS * DM * 2);
  u16* qkv = (u16*)take((size_t)MROWS * 3 * DM * 2);
  u16* E = (u16*)take((size_t)4 * NSEQ * NSEQ * 2);
  f32x2* st = (f32x2*)take((size_t)4 * NSEQ * 8 * 8);
  u16* O = (u16*)take((size_t)MROWS * DM * 2);
  u16* x1 = (u16*)take((size_t)MROWS * DM * 2);
  u16* h2 = (u16*)take((size_t)MROWS * DM * 2);
  u16* f = (u16*)take((size_t)MROWS * FF * 2);
  float* part = (float*)take((size_t)4 * MROWS * DM * 4);

  constexpr int LDS_QKV = 4 * GC<128, 144, 4, 1, 4, true>::STAGE;
  constexpr int LDS_SC = 4 * GC<128, 128, 2, 2, 4, false>::STAGE;
  constexpr int LDS_PV = 5 * GC<64, 96, 2, 2, 5, true>::STAGE + 64 * 8 * 4;
  constexpr int LDS_WO = 5 * GC<64, 96, 2, 2, 5, true>::STAGE;
  constexpr int LDS_FC = 4 * GC<128, 192, 2, 2, 4, true>::STAGE;
  hipFuncSetAttribute((const void*)k_qkv, hipFuncAttributeMaxDynamicSharedMemorySize, LDS_QKV);
  hipFuncSetAttribute((const void*)k_scores, hipFuncAttributeMaxDynamicSharedMemorySize, LDS_SC);
  hipFuncSetAttribute((const void*)k_pv, hipFuncAttributeMaxDynamicSharedMemorySize, LDS_PV);
  hipFuncSetAttribute((const void*)k_wo, hipFuncAttributeMaxDynamicSharedMemorySize, LDS_WO);
  hipFuncSetAttribute((const void*)k_fc1, hipFuncAttributeMaxDynamicSharedMemorySize, LDS_FC);
  hipFuncSetAttribute((const void*)k_fc2, hipFuncAttributeMaxDynamicSharedMemorySize, LDS_FC);

  k_mod<<<144, 256, 0, stream>>>(c, wmod, mod);
  k_lnmod<<<MROWS / 4, 256, 0, stream>>>(x, mod, 0, h);
  k_qkv<<<256, 256, LDS_QKV, stream>>>(h, wqkv, qkv);
  k_scores<<<256, 256, LDS_SC, stream>>>(qkv, E, st);
  k_pv<<<256, 256, LDS_PV, stream>>>(E, st, qkv, O);
  k_wo<<<256, 256, LDS_WO, stream>>>(O, wo, x, mod, x1);
  k_lnmod<<<MROWS / 4, 256, 0, stream>>>(x1, mod, 3, h2);
  k_fc1<<<256, 256, LDS_FC, stream>>>(h2, w1, f);
  k_fc2<<<256, 256, LDS_FC, stream>>>(f, w2, part);
  k_final<<<MROWS * DM / 8 / 256, 256, 0, stream>>>(part, x1, mod, out);
}
```

```cpp
#include <hip/hip_runtime.h>
#include <hip/hip_bf16.h>
#include <stdint.h>

typedef unsigned short u16;
typedef __attribute__((ext_vector_type(8))) __bf16 bf16x8;
typedef __attribute__((ext_vector_type(4))) __bf16 bf16x4;
typedef __attribute__((ext_vector_type(2))) __bf16 bf16x2;
typedef __attribute__((ext_vector_type(4))) float f32x4;
typedef __attribute__((ext_vector_type(2))) float f32x2;
typedef __attribute__((ext_vector_type(4))) unsigned u32x4;
typedef __attribute__((ext_vector_type(2))) unsigned u32x2;
#define LAS __attribute__((address_space(3)))
#define GAS __attribute__((address_space(1)))

constexpr int NB = 2, NSEQ = 1024, DM = 768, NH = 2, DH = 384, FF = 3072;
constexpr int MROWS = NB * NSEQ;

__device__ __forceinline__ float bf2f(u16 v) { return __uint_as_float(((unsigned)v) << 16); }
__device__ __forceinline__ float bflo(unsigned v) { return __uint_as_float(v << 16); }
__device__ __forceinline__ float bfhi(unsigned v) { return __uint_as_float(v & 0xffff0000u); }
__device__ __forceinline__ unsigned pack2(float a, float b) {
  f32x2 f = {a, b};
  bf16x2 r = __builtin_convertvector(f, bf16x2);
  return __builtin_bit_cast(unsigned, r);
}
__device__ __forceinline__ float rbf(float a) {
  return bflo(pack2(a, 0.f) & 0xffffu);
}

template <int N>
__device__ __forceinline__ void waitv() {
  asm volatile("s_waitcnt vmcnt(%0)" ::"i"(N) : "memory");
}
template <int I>
struct IC {
  static constexpr int value = I;
};
template <int B, int E, class F>
__device__ __forceinline__ void static_for(F&& f) {
  if constexpr (B < E) {
    f(IC<B>{});
    static_for<B + 1, E>(f);
  }
}
#define FENCE() asm volatile("" ::: "memory")
#define STAMP(kid, i)
#define BARRIER()                        \
  do {                                   \
    FENCE();                             \
    __builtin_amdgcn_s_barrier();        \
    FENCE();                             \
  } while (0)

template <int BM, int BN, int WM, int WN, int KG, int NS, bool BKN>
struct GC {
  static constexpr int NTW = WM * WN;
  static constexpr int NW = NTW * KG;
  static constexpr int KH = KG / 2;
  static constexpr int WTM = BM / WM, WTN = BN / WN, MT = WTM / 16, NT = WTN / 16;
  static constexpr int A_BYTES = BM * 128, B_BYTES = BN * 128, HALF = A_BYTES + B_BYTES, STAGE = KH * HALF;
  static constexpr int NA = BM / 8, NBI = BN / 8;
  static constexpr int LA = KH * NA / NW;
  static constexpr int LB = (KH * NBI + NW - 1) / NW;
  static constexpr int NFULL = (KH * NBI) % NW == 0 ? NW : (KH * NBI) % NW;
  static constexpr int L = LA + LB;
  static constexpr int RED_BYTES = (KG - 1) * BM * BN * 4;
  static constexpr int RS = BN * 2 + 16;
  static constexpr int LDS_MAIN = NS * STAGE;
  static constexpr int LDS_EPI = RED_BYTES + BM * RS;
  static constexpr int LDS = LDS_MAIN > LDS_EPI ? LDS_MAIN : LDS_EPI;
  static_assert(NW == 8, "8 waves");
  static_assert((KH * NA) % NW == 0, "A instr split");
  static_assert(NS >= 3, "stages");
};

template <int P>
__device__ __forceinline__ void l2_touch(const char* base, size_t row_stride, int row0, int rowmul, int nrows,
                                         int lpr, int seg_bytes, char* lds_dummy) {
#pragma unroll
  for (int j = 0; j < P; ++j) {
    int idx = threadIdx.x + j * 512;
    int r = idx / lpr, ln = idx % lpr;
    r = r < nrows ? r : 0;
    int off = ln * 128;
    off = off < seg_bytes - 4 ? off : seg_bytes - 4;
    const char* p = base + (size_t)(row0 + r * rowmul) * row_stride + off;
    __builtin_amdgcn_global_load_lds((const GAS unsigned*)p, (LAS unsigned*)lds_dummy, 4, 0, 0);
  }
}

template <int BM, int BN, int WM, int WN, int KG, int NS, bool BKN, int T, int PF, int KID, class Pre, class Hook>
__device__ __forceinline__ void gemm_main(const u16* __restrict__ Ab, int lda, const u16* __restrict__ Bb, int ldb,
                                          char* smem, f32x4 (&acc)[BM / WM / 16][BN / WN / 16], Pre pre, Hook hook) {
  using C = GC<BM, BN, WM, WN, KG, NS, BKN>;
  constexpr int MT = C::MT, NT = C::NT, L = C::L, LA = C::LA, LB = C::LB, NW = C::NW, KH = C::KH;
  const int tid = threadIdx.x, lane = tid & 63;
  const int wave = __builtin_amdgcn_readfirstlane(tid >> 6);
  const int w4 = wave % C::NTW, kg = wave / C::NTW;
  const int wm = w4 % WM, wn = w4 / WM;
  const bool full = (C::NFULL == NW) || (wave < C::NFULL);

  unsigned goffA[LA], goffB[LB], ldsA[LA], ldsB[LB];
#pragma unroll
  for (int i = 0; i < LA; ++i) {
    int q = i * NW + wave;
    int half = q / C::NA, r = q % C::NA;
    int row = r * 8 + (lane >> 3);
    int lc = (lane & 7) ^ (row & 7);
    goffA[i] = (unsigned)(row * lda + half * 64 + lc * 8) * 2u;
    ldsA[i] = (unsigned)(half * C::HALF + r * 1024);
  }
#pragma unroll
  for (int i = 0; i < LB; ++i) {
    int q = i * NW + wave;
    if (q >= KH * C::NBI) q = 0;
    int half = q / C::NBI, r = q % C::NBI;
    ldsB[i] = (unsigned)(half * C::HALF + C::A_BYTES + r * 1024);
    if (BKN) {
      int st = r * 4 + (lane >> 4);
      int kb = st / (BN / 16), nb = st % (BN / 16);
      int i16 = lane & 15, prow = i16 >> 1, hf = i16 & 1;
      int lrow = prow ^ ((kb & 1) << 2);
      goffB[i] = (unsigned)((half * 64 + kb * 8 + lrow) * ldb + nb * 16 + hf * 8) * 2u;
    } else {
      int row = r * 8 + (lane >> 3);
      int lc = (lane & 7) ^ (row & 7);
      goffB[i] = (unsigned)(row * ldb + half * 64 + lc * 8) * 2u;
    }
  }
  const char* Abase = (const char*)Ab;
  const char* Bbase = (const char*)Bb;
  const size_t astep = (size_t)KH * 128;
  const size_t bstep = BKN ? (size_t)KH * 64 * ldb * 2 : (size_t)KH * 128;

  auto issue = [&](int t) {
    char* sb = smem + (t % NS) * C::STAGE;
    const char* pa = Abase + (size_t)t * astep;
    const char* pb = Bbase + (size_t)t * bstep;
#pragma unroll
    for (int i = 0; i < LA; ++i)
      __builtin_amdgcn_global_load_lds((const GAS unsigned*)(pa + goffA[i]), (LAS unsigned*)(sb + ldsA[i]), 16, 0, 0);
#pragma unroll
    for (int i = 0; i < LB; ++i)
      if (i + 1 < LB || full)
        __builtin_amdgcn_global_load_lds((const GAS unsigned*)(pb + goffB[i]), (LAS unsigned*)(sb + ldsB[i]), 16, 0,
                                         0);
  };
  auto wait_st = [&](auto NN, auto XX) {
    constexpr int n = NN.value, x = XX.value;
    if (C::NFULL == NW || n == 0) {
      waitv<n * L + x>();
    } else {
      if (full) waitv<n * L + x>();
      else waitv<n * (L - 1) + x>();
    }
  };

  const int g = lane >> 4, i16 = lane & 15;
  const int khalf = kg >> 1, s = kg & 1;
  const unsigned aoff = (unsigned)(khalf * C::HALF + (wm * C::WTM + i16) * 128 + (((s * 4 + g) ^ (lane & 7)) << 4));
  unsigned boff[2];
  if (BKN) {
    const int q4 = i16 >> 2, p = i16 & 3;
#pragma unroll
    for (int h = 0; h < 2; ++h)
      boff[h] = (unsigned)(khalf * C::HALF + C::A_BYTES + ((s * 4 + g) * (BN / 16) + wn * NT) * 256 +
                           (((h * 4 + q4) ^ ((g & 1) << 2)) * 32) + p * 8);
  } else {
    boff[0] = boff[1] =
        (unsigned)(khalf * C::HALF + C::A_BYTES + (wn * C::WTN + i16) * 128 + (((s * 4 + g) ^ (lane & 7)) << 4));
  }

  auto rd_frag = [&](int slot, bf16x8 (&fa)[MT], bf16x8 (&fb)[NT]) {
    const char* sb = smem + slot * C::STAGE;
#pragma unroll
    for (int m = 0; m < MT; ++m) fa[m] = *(const bf16x8*)(sb + aoff + m * 2048);
#pragma unroll
    for (int n = 0; n < NT; ++n) {
      if (BKN) {
        bf16x4 lo = __builtin_amdgcn_ds_read_tr16_b64_v4bf16((LAS bf16x4*)(sb + boff[0] + n * 256));
        bf16x4 hi = __builtin_amdgcn_ds_read_tr16_b64_v4bf16((LAS bf16x4*)(sb + boff[1] + n * 256));
        fb[n] = __builtin_shufflevector(lo, hi, 0, 1, 2, 3, 4, 5, 6, 7);
      } else {
        fb[n] = *(const bf16x8*)(sb + boff[0] + n * 2048);
      }
    }
  };
  auto mma = [&](bf16x8 (&fa)[MT], bf16x8 (&fb)[NT]) {
#pragma unroll
    for (int m = 0; m < MT; ++m)
#pragma unroll
      for (int n = 0; n < NT; ++n)
        acc[m][n] = __builtin_amdgcn_mfma_f32_16x16x32_bf16(fb[n], fa[m], acc[m][n], 0, 0, 0);
  };

  STAMP(KID, 0);
  issue(0);
  pre(smem + (NS - 1) * C::STAGE);
  static_for<1, NS - 1>([&](auto S) { issue(S.value); });
  wait_st(IC<NS - 2>{}, IC<PF>{});
  BARRIER();
  STAMP(KID, 1);
  bf16x8 fa0[MT], fb0[NT], fa1[MT], fb1[NT];
  rd_frag(0, fa0, fb0);
  static_for<0, T>([&](auto TT) {
    constexpr int t = TT.value;
    if constexpr (t + 1 < T) {
      constexpr int rem = (T - 2 - t) < (NS - 3) ? (T - 2 - t) : (NS - 3);
      wait_st(IC<rem>{}, IC<0>{});
      BARRIER();
      if constexpr (t + NS - 1 < T) issue(t + NS - 1);
      if constexpr ((t & 1) == 0) rd_frag((t + 1) % NS, fa1, fb1);
      else rd_frag((t + 1) % NS, fa0, fb0);
    }
    __builtin_amdgcn_sched_barrier(0);
    if constexpr ((t & 1) == 0) mma(fa0, fb0);
    else mma(fa1, fb1);
    hook(TT);
    __builtin_amdgcn_sched_barrier(0);
  });
  STAMP(KID, 2);
}

struct NoHook {
  template <class X>
  __device__ __forceinline__ void operator()(X) const {}
};

template <int BM, int BN, int WM, int WN, int KG, bool STAGE_BF16>
__device__ __forceinline__ void finish_acc(char* smem, f32x4 (&acc)[BM / WM / 16][BN / WN / 16]) {
  using C = GC<BM, BN, WM, WN, KG, 3, true>;
  constexpr int MT = C::MT, NT = C::NT;
  const int tid = threadIdx.x, lane = tid & 63;
  const int wave = __builtin_amdgcn_readfirstlane(tid >> 6);
  const int w4 = wave % C::NTW, kg = wave / C::NTW;
  const int wm = w4 % WM, wn = w4 / WM;
  BARRIER();
  if (kg > 0) {
    char* r = smem + ((kg - 1) * C::NTW + w4) * (MT * NT * 1024) + lane * 16;
#pragma unroll
    for (int m = 0; m < MT; ++m)
#pragma unroll
      for (int n = 0; n < NT; ++n) *(f32x4*)(r + (m * NT + n) * 1024) = acc[m][n];
  }
  __syncthreads();
  if (kg == 0) {
#pragma unroll
    for (int gq = 0; gq < KG - 1; ++gq) {
      const char* r = smem + (gq * C::NTW + w4) * (MT * NT * 1024) + lane * 16;
#pragma unroll
      for (int m = 0; m < MT; ++m)
#pragma unroll
        for (int n = 0; n < NT; ++n) acc[m][n] += *(const f32x4*)(r + (m * NT + n) * 1024);
    }
    if (STAGE_BF16) {
      char* stg = smem + C::RED_BYTES;
#pragma unroll
      for (int m = 0; m < MT; ++m)
#pragma unroll
        for (int n = 0; n < NT; ++n) {
          int row = wm * C::WTM + m * 16 + (lane & 15);
          int col = wn * C::WTN + n * 16 + (lane >> 4) * 4;
          u32x2 v = {pack2(acc[m][n][0], acc[m][n][1]), pack2(acc[m][n][2], acc[m][n][3])};
          *(u32x2*)(stg + row * C::RS + col * 2) = v;
        }
    }
  }
  if (STAGE_BF16) __syncthreads();
}

template <int CTRL>
__device__ __forceinline__ float dppf(float x) {
  return __builtin_bit_cast(float, __builtin_amdgcn_update_dpp(0, __builtin_bit_cast(int, x), CTRL, 0xf, 0xf, true));
}
__device__ __forceinline__ float red16_max(float x) {
  x = fmaxf(x, dppf<0xB1>(x));
  x = fmaxf(x, dppf<0x4E>(x));
  x = fmaxf(x, dppf<0x124>(x));
  x = fmaxf(x, dppf<0x128>(x));
  return x;
}
__device__ __forceinline__ float red16_sum(float x) {
  x += dppf<0xB1>(x);
  x += dppf<0x4E>(x);
  x += dppf<0x124>(x);
  x += dppf<0x128>(x);
  return x;
}

__global__ __launch_bounds__(256) void k_mod(const u16* __restrict__ c, const u16* __restrict__ wmod,
                                             float* __restrict__ mod) {
  __shared__ float red[2][64][33];
  const int tid = threadIdx.x, cg = tid & 3, kq = tid >> 2;
  const int col0 = blockIdx.x * 32 + cg * 8;
  u32x4 w[12];
#pragma unroll
  for (int i = 0; i < 12; ++i) {
    int k = i * 64 + kq;
    w[i] = *(const u32x4*)(wmod + (size_t)k * (6 * DM) + col0);
  }
  float a0[8], a1[8];
#pragma unroll
  for (int j = 0; j < 8; ++j) a0[j] = a1[j] = 0.f;
#pragma unroll
  for (int i = 0; i < 12; ++i) {
    int k = i * 64 + kq;
    float c0 = bf2f(c[k]), c1 = bf2f(c[DM + k]);
    float s0 = rbf(c0 / (1.f + __expf(-c0)));
    float s1 = rbf(c1 / (1.f + __expf(-c1)));
#pragma unroll
    for (int j = 0; j < 4; ++j) {
      float wl = bflo(w[i][j]), wh = bfhi(w[i][j]);
      a0[2 * j] += s0 * wl; a0[2 * j + 1] += s0 * wh;
      a1[2 * j] += s1 * wl; a1[2 * j + 1] += s1 * wh;
    }
  }
#pragma unroll
  for (int j = 0; j < 8; ++j) {
    red[0][kq][cg * 8 + j] = a0[j];
    red[1][kq][cg * 8 + j] = a1[j];
  }
  __syncthreads();
  if (tid < 64) {
    int b = tid >> 5, col = tid & 31;
    float s = 0.f;
    for (int q = 0; q < 64; ++q) s += red[b][q][col];
    mod[b * (6 * DM) + blockIdx.x * 32 + col] = rbf(s);
  }
}

__global__ __launch_bounds__(256) void k_lnmod(const u16* __restrict__ x, const float* __restrict__ mod,
                                               int shift_idx, u16* __restrict__ out) {
  const int lane = threadIdx.x & 63, row = blockIdx.x * 4 + (threadIdx.x >> 6);
  const int b = row >> 10;
  const u16* xr = x + (size_t)row * DM;
  float v[12];
#pragma unroll
  for (int i = 0; i < 3; ++i) {
    u32x2 u = *(const u32x2*)(xr + i * 256 + lane * 4);
    v[i * 4 + 0] = bflo(u[0]); v[i * 4 + 1] = bfhi(u[0]);
    v[i * 4 + 2] = bflo(u[1]); v[i * 4 + 3] = bfhi(u[1]);
  }
  float s = 0.f;
#pragma unroll
  for (int i = 0; i < 12; ++i) s += v[i];
#pragma unroll
  for (int o = 32; o; o >>= 1) s += __shfl_xor(s, o);
  const float mean = s * (1.f / DM);
  float q = 0.f;
#pragma unroll
  for (int i = 0; i < 12; ++i) { float d = v[i] - mean; q += d * d; }
#pragma unroll
  for (int o = 32; o; o >>= 1) q += __shfl_xor(q, o);
  const float mb = rbf(mean), vb = rbf(q * (1.f / DM));
  const float sd = rbf(__fsqrt_rn(rbf(vb + 1e-5f)));
  const float* shift = mod + b * (6 * DM) + shift_idx * DM;
  const float* scale = shift + DM;
#pragma unroll
  for (int i = 0; i < 3; ++i) {
    int c0 = i * 256 + lane * 4;
    f32x4 sc = *(const f32x4*)(scale + c0), sh = *(const f32x4*)(shift + c0);
    float h[4];
#pragma unroll
    for (int j = 0; j < 4; ++j) {
      float n = rbf(rbf(v[i * 4 + j] - mb) / sd);
      h[j] = rbf(n * rbf(1.f + sc[j])) + sh[j];
    }
    u32x2 o = {pack2(h[0], h[1]), pack2(h[2], h[3])};
    *(u32x2*)(out + (size_t)row * DM + c0) = o;
  }
}

using GQKV = GC<128, 144, 4, 1, 2, 4, true>;
__global__ __launch_bounds__(512, 2) void k_qkv(const u16* __restrict__ h, const u16* __restrict__ w,
                                                u16* __restrict__ qkv) {
  extern __shared__ __attribute__((aligned(16))) char smem[];
  constexpr int BM = 128, BN = 144, WM = 4, WN = 1, KG = 2, NS = 4;
  const int x = blockIdx.x & 7, j = blockIdx.x >> 3;
  const int tm = (x >> 2) * 8 + (j >> 2), tn = (x & 3) * 4 + (j & 3);
  const int m0 = tm * BM, n0 = tn * BN;
  const u16* A = h + (size_t)m0 * DM;
  const u16* B = w + n0;
  auto pre = [&](char* dummy) {
    l2_touch<1>((const char*)A, DM * 2, j & 3, 4, 32, 12, DM * 2, dummy);
    l2_touch<1>((const char*)B, 3 * DM * 2, j >> 2, 8, 96, 4, BN * 2, dummy);
  };
  f32x4 acc[2][9] = {};
  gemm_main<BM, BN, WM, WN, KG, NS, true, 12, 2, 0>(A, DM, B, 3 * DM, smem, acc, pre, NoHook());
  finish_acc<BM, BN, WM, WN, KG, true>(smem, acc);
  constexpr int CPR = BN / 8;
  const char* stg = smem + GQKV::RED_BYTES;
  for (int c = threadIdx.x; c < BM * CPR; c += 512) {
    int row = c / CPR, c8 = c % CPR;
    u32x4 v = *(const u32x4*)(stg + row * GQKV::RS + c8 * 16);
    *(u32x4*)(qkv + (size_t)(m0 + row) * (3 * DM) + n0 + c8 * 8) = v;
  }
  STAMP(0, 3);
}

using GSC = GC<128, 128, 2, 2, 2, 4, false>;
__global__ __launch_bounds__(512, 2) void k_scores(const u16* __restrict__ qkv, u16* __restrict__ E,
                                                   f32x2* __restrict__ st) {
  extern __shared__ __attribute__((aligned(16))) char smem[];
  constexpr int BM = 128, BN = 128, WM = 2, WN = 2, KG = 2, NS = 4;
  const int x = blockIdx.x & 7, j = blockIdx.x >> 3;
  const int bh = x >> 1, b = bh >> 1, hh = bh & 1;
  const int tm = (x & 1) * 4 + (j >> 3), tn = j & 7;
  const int m0 = tm * BM, n0 = tn * BN;
  const u16* Q = qkv + (size_t)(b * NSEQ + m0) * (3 * DM) + hh * DH;
  const u16* K = qkv + (size_t)(b * NSEQ + n0) * (3 * DM) + DM + hh * DH;
  auto pre = [&](char* dummy) {
    l2_touch<1>((const char*)Q, 3 * DM * 2, j & 7, 8, 16, 6, DH * 2, dummy);
    l2_touch<1>((const char*)K, 3 * DM * 2, j >> 3, 4, 32, 6, DH * 2, dummy);
  };
  f32x4 acc[4][4] = {};
  gemm_main<BM, BN, WM, WN, KG, NS, false, 6, 2, 1>(Q, 3 * DM, K, 3 * DM, smem, acc, pre, NoHook());
  finish_acc<BM, BN, WM, WN, KG, true>(smem, acc);
  const char* stg = smem + GSC::RED_BYTES;
  const float scale = 0.05103103630798288f;
  const int l16 = threadIdx.x & 15, r0 = threadIdx.x >> 4;
#pragma unroll
  for (int i = 0; i < 4; ++i) {
    int row = r0 + i * 32;
    u32x4 v = *(const u32x4*)(stg + row * GSC::RS + l16 * 16);
    float s[8];
#pragma unroll
    for (int e = 0; e < 4; ++e) {
      unsigned pk = pack2(bflo(v[e]) * scale, bfhi(v[e]) * scale);
      s[2 * e] = bflo(pk);
      s[2 * e + 1] = bfhi(pk);
    }
    float mx = fmaxf(fmaxf(fmaxf(s[0], s[1]), fmaxf(s[2], s[3])), fmaxf(fmaxf(s[4], s[5]), fmaxf(s[6], s[7])));
    mx = red16_max(mx);
    float sum = 0.f;
#pragma unroll
    for (int e = 0; e < 8; ++e) {
      s[e] = __expf(s[e] - mx);
      sum += s[e];
    }
    sum = red16_sum(sum);
    u32x4 o4 = {pack2(s[0], s[1]), pack2(s[2], s[3]), pack2(s[4], s[5]), pack2(s[6], s[7])};
    *(u32x4*)(E + ((size_t)bh * NSEQ + m0 + row) * NSEQ + n0 + l16 * 8) = o4;
    if (l16 == 0) st[((size_t)bh * NSEQ + m0 + row) * 8 + tn] = f32x2{mx, sum};
  }
  STAMP(1, 3);
}

using GPV = GC<64, 96, 2, 1, 4, 3, true>;
__global__ __launch_bounds__(512, 2) void k_pv(const u16* __restrict__ E, const f32x2* __restrict__ st,
                                               const u16* __restrict__ qkv, u16* __restrict__ O) {
  extern __shared__ __attribute__((aligned(16))) char smem[];
  constexpr int BM = 64, BN = 96, WM = 2, WN = 1, KG = 4, NS = 3;
  const int x = blockIdx.x & 7, j = blockIdx.x >> 3;
  const int bh = x >> 1, b = bh >> 1, hh = bh & 1;
  const int tm = (x & 1) * 8 + (j >> 2), tn = j & 3;
  const int m0 = tm * BM, n0 = tn * BN;
  float* ftab = (float*)(smem + GPV::LDS);
  const u16* A = E + ((size_t)bh * NSEQ + m0) * NSEQ;
  const u16* V = qkv + (size_t)(b * NSEQ) * (3 * DM) + 2 * DM + hh * DH + n0;
  auto pre = [&](char* dummy) {
    l2_touch<1>((const char*)A, NSEQ * 2, j & 3, 4, 16, 16, NSEQ * 2, dummy);
    l2_touch<1>((const char*)V, 3 * DM * 2, j >> 2, 8, 128, 3, BN * 2, dummy);
    const int tid = threadIdx.x;
    if (tid < 64) {
      const f32x2* p = st + ((size_t)bh * NSEQ + m0 + tid) * 8;
      f32x2 v[8];
#pragma unroll
      for (int t = 0; t < 8; ++t) v[t] = p[t];
      float M = v[0][0];
#pragma unroll
      for (int t = 1; t < 8; ++t) M = fmaxf(M, v[t][0]);
      float e[8], Ls = 0.f;
#pragma unroll
      for (int t = 0; t < 8; ++t) {
        e[t] = __expf(v[t][0] - M);
        Ls += e[t] * v[t][1];
      }
      float inv = 1.f / Ls;
#pragma unroll
      for (int t = 0; t < 8; ++t) ftab[tid * 8 + t] = e[t] * inv;
      asm volatile("s_waitcnt lgkmcnt(0)" ::: "memory");
    }
  };
  f32x4 acc[2][6] = {};
  f32x4 tot[2][6] = {};
  const int lane = threadIdx.x & 63, wave = threadIdx.x >> 6, wm = wave % WM;
  auto hook = [&](auto TT) {
    constexpr int t = TT.value;
#pragma unroll
    for (int m = 0; m < 2; ++m) {
      float f = ftab[(wm * 32 + m * 16 + (lane & 15)) * 8 + t];
#pragma unroll
      for (int n = 0; n < 6; ++n) {
        tot[m][n] += acc[m][n] * f;
        acc[m][n] = f32x4{0.f, 0.f, 0.f, 0.f};
      }
    }
  };
  gemm_main<BM, BN, WM, WN, KG, NS, true, 8, 2, 2>(A, NSEQ, V, 3 * DM, smem, acc, pre, hook);
  finish_acc<BM, BN, WM, WN, KG, true>(smem, tot);
  constexpr int CPR = BN / 8;
  const char* stg = smem + GPV::RED_BYTES;
  for (int c = threadIdx.x; c < BM * CPR; c += 512) {
    int row = c / CPR, c8 = c % CPR;
    u32x4 v = *(const u32x4*)(stg + row * GPV::RS + c8 * 16);
    *(u32x4*)(O + (size_t)(b * NSEQ + m0 + row) * DM + hh * DH + n0 + c8 * 8) = v;
  }
  STAMP(2, 3);
}

__global__ __launch_bounds__(512, 2) void k_wo(const u16* __restrict__ O, const u16* __restrict__ wo,
                                               const u16* __restrict__ xin, const float* __restrict__ mod,
                                               u16* __restrict__ x1) {
  extern __shared__ __attribute__((aligned(16))) char smem[];
  constexpr int BM = 64, BN = 96, WM = 2, WN = 1, KG = 4, NS = 3;
  const int x = blockIdx.x & 7, j = blockIdx.x >> 3;
  const int tm = (x >> 1) * 8 + (j >> 2), tn = (x & 1) * 4 + (j & 3);
  const int m0 = tm * BM, n0 = tn * BN, b = m0 >> 10;
  const u16* A = O + (size_t)m0 * DM;
  const u16* B = wo + n0;
  auto pre = [&](char* dummy) {
    l2_touch<1>((const char*)A, DM * 2, j & 3, 4, 16, 12, DM * 2, dummy);
    l2_touch<1>((const char*)B, DM * 2, j >> 2, 8, 96, 3, BN * 2, dummy);
  };
  f32x4 acc[2][6] = {};
  gemm_main<BM, BN, WM, WN, KG, NS, true, 6, 2, 3>(A, DM, B, DM, smem, acc, pre, NoHook());
  finish_acc<BM, BN, WM, WN, KG, true>(smem, acc);
  constexpr int CPR = BN / 8;
  const char* stg = smem + GPV::RED_BYTES;
  const float* gate = mod + b * (6 * DM) + 2 * DM + n0;
  for (int c = threadIdx.x; c < BM * CPR; c += 512) {
    int row = c / CPR, c8 = c % CPR;
    u32x4 v = *(const u32x4*)(stg + row * GPV::RS + c8 * 16);
    u32x4 xv = *(const u32x4*)(xin + (size_t)(m0 + row) * DM + n0 + c8 * 8);
    f32x4 g0 = *(const f32x4*)(gate + c8 * 8), g1 = *(const f32x4*)(gate + c8 * 8 + 4);
    u32x4 o;
#pragma unroll
    for (int e = 0; e < 4; ++e) {
      float ga = e < 2 ? g0[2 * e] : g1[2 * e - 4], gb = e < 2 ? g0[2 * e + 1] : g1[2 * e - 3];
      unsigned pk = pack2(ga * bflo(v[e]), gb * bfhi(v[e]));
      o[e] = pack2(bflo(xv[e]) + bflo(pk), bfhi(xv[e]) + bfhi(pk));
    }
    *(u32x4*)(x1 + (size_t)(m0 + row) * DM + n0 + c8 * 8) = o;
  }
  STAMP(3, 3);
}

__device__ __forceinline__ float gelu_tanh(float u) {
  float w = u * (-2.f * 0.7978845608028654f * 1.4426950408889634f);
  float a = w * (1.f + 0.044715f * u * u);
  return u * __builtin_amdgcn_rcpf(1.f + __builtin_amdgcn_exp2f(a));
}
using GFC = GC<128, 192, 2, 2, 2, 4, true>;
__global__ __launch_bounds__(512, 2) void k_fc1(const u16* __restrict__ h2, const u16* __restrict__ w1,
                                                u16* __restrict__ f) {
  extern __shared__ __attribute__((aligned(16))) char smem[];
  constexpr int BM = 128, BN = 192, WM = 2, WN = 2, KG = 2, NS = 4;
  const int x = blockIdx.x & 7, j = blockIdx.x >> 3;
  const int tm = (x >> 2) * 8 + (j >> 2), tn = (x & 3) * 4 + (j & 3);
  const int m0 = tm * BM, n0 = tn * BN;
  const u16* A = h2 + (size_t)m0 * DM;
  const u16* B = w1 + n0;
  auto pre = [&](char* dummy) {
    l2_touch<1>((const char*)A, DM * 2, j & 3, 4, 32, 12, DM * 2, dummy);
    l2_touch<1>((const char*)B, FF * 2, j >> 2, 8, 96, 3, BN * 2, dummy);
  };
  f32x4 acc[4][6] = {};
  gemm_main<BM, BN, WM, WN, KG, NS, true, 12, 2, 4>(A, DM, B, FF, smem, acc, pre, NoHook());
  finish_acc<BM, BN, WM, WN, KG, true>(smem, acc);
  constexpr int CPR = BN / 8;
  const char* stg = smem + GFC::RED_BYTES;
#pragma unroll 2
  for (int c = threadIdx.x; c < BM * CPR; c += 512) {
    int row = c / CPR, c8 = c % CPR;
    u32x4 v = *(const u32x4*)(stg + row * GFC::RS + c8 * 16);
    u32x4 o;
#pragma unroll
    for (int e = 0; e < 4; ++e) o[e] = pack2(gelu_tanh(bflo(v[e])), gelu_tanh(bfhi(v[e])));
    *(u32x4*)(f + (size_t)(m0 + row) * FF + n0 + c8 * 8) = o;
  }
  STAMP(4, 3);
}

__global__ __launch_bounds__(512, 2) void k_fc2(const u16* __restrict__ f, const u16* __restrict__ w2,
                                                float* __restrict__ part) {
  extern __shared__ __attribute__((aligned(16))) char smem[];
  constexpr int BM = 128, BN = 192, WM = 2, WN = 2, KG = 2, NS = 4;
  const int x = blockIdx.x & 7, j = blockIdx.x >> 3;
  const int ks = x >> 1, tm = (x & 1) * 8 + (j >> 2), tn = j & 3;
  const int m0 = tm * BM, n0 = tn * BN;
  const u16* A = f + (size_t)m0 * FF + ks * 768;
  const u16* B = w2 + (size_t)ks * 768 * DM + n0;
  auto pre = [&](char* dummy) {
    l2_touch<1>((const char*)A, FF * 2, j & 3, 4, 32, 12, DM * 2, dummy);
    l2_touch<1>((const char*)B, DM * 2, j >> 2, 8, 96, 3, BN * 2, dummy);
  };
  f32x4 acc[4][6] = {};
  gemm_main<BM, BN, WM, WN, KG, NS, true, 12, 2, 5>(A, FF, B, DM, smem, acc, pre, NoHook());
  finish_acc<BM, BN, WM, WN, KG, false>(smem, acc);
  const int lane = threadIdx.x & 63, wave = threadIdx.x >> 6, wm = wave % WM, wn = (wave / WM) % WN;
  if (wave < 4) {
    float* P = part + (size_t)ks * MROWS * DM;
#pragma unroll
    for (int m = 0; m < 4; ++m)
#pragma unroll
      for (int n = 0; n < 6; ++n) {
        int row = m0 + wm * 64 + m * 16 + (lane & 15);
        int col = n0 + wn * 96 + n * 16 + (lane >> 4) * 4;
        *(f32x4*)(P + (size_t)row * DM + col) = acc[m][n];
      }
  }
  STAMP(5, 3);
}

__global__ __launch_bounds__(256) void k_final(const float* __restrict__ part, const u16* __restrict__ x1,
                                               const float* __restrict__ mod, u16* __restrict__ out) {
  const int idx = blockIdx.x * 256 + threadIdx.x;
  const int row = idx / (DM / 8), c8 = idx % (DM / 8), b = row >> 10;
  const size_t off = (size_t)row * DM + c8 * 8;
  f32x4 s0 = {0.f, 0.f, 0.f, 0.f}, s1 = s0;
#pragma unroll
  for (int k = 0; k < 4; ++k) {
    s0 += *(const f32x4*)(part + (size_t)k * MROWS * DM + off);
    s1 += *(const f32x4*)(part + (size_t)k * MROWS * DM + off + 4);
  }
  const float* gate = mod + b * (6 * DM) + 5 * DM + c8 * 8;
  f32x4 g0 = *(const f32x4*)gate, g1 = *(const f32x4*)(gate + 4);
  u32x4 xv = *(const u32x4*)(x1 + off);
  u32x4 o;
#pragma unroll
  for (int e = 0; e < 4; ++e) {
    float ma = e < 2 ? s0[2 * e] : s1[2 * e - 4], mb = e < 2 ? s0[2 * e + 1] : s1[2 * e - 3];
    float ga = e < 2 ? g0[2 * e] : g1[2 * e - 4], gb = e < 2 ? g0[2 * e + 1] : g1[2 * e - 3];
    float r0 = bflo(xv[e]) + rbf(ga * rbf(ma));
    float r1 = bfhi(xv[e]) + rbf(gb * rbf(mb));
    o[e] = pack2(r0, r1);
  }
  *(u32x4*)(out + off) = o;
}

extern "C" void kernel_launch(void* const* d_in, const int* in_sizes, int n_in, void* d_out, int out_size,
                              void* d_ws, size_t ws_size, hipStream_t stream) {
  const u16* x = (const u16*)d_in[0];
  const u16* c = (const u16*)d_in[1];
  const u16* wqkv = (const u16*)d_in[2];
  const u16* wo = (const u16*)d_in[3];
  const u16* wmod = (const u16*)d_in[4];
  const u16* w1 = (const u16*)d_in[5];
  const u16* w2 = (const u16*)d_in[6];
  u16* out = (u16*)d_out;

  char* ws = (char*)d_ws;
  size_t o = 0;
  auto take = [&](size_t bytes) { char* p = ws + o; o += (bytes + 255) & ~(size_t)255; return p; };
  float* mod = (float*)take(2 * 6 * DM * 4);
  u16* h = (u16*)take((size_t)MROWS * DM * 2);
  u16* qkv = (u16*)take((size_t)MROWS * 3 * DM * 2);
  u16* E = (u16*)take((size_t)4 * NSEQ * NSEQ * 2);
  f32x2* st = (f32x2*)take((size_t)4 * NSEQ * 8 * 8);
  u16* O = (u16*)take((size_t)MROWS * DM * 2);
  u16* x1 = (u16*)take((size_t)MROWS * DM * 2);
  u16* h2 = (u16*)take((size_t)MROWS * DM * 2);
  u16* f = (u16*)take((size_t)MROWS * FF * 2);
  float* part = (float*)take((size_t)4 * MROWS * DM * 4);

  constexpr int LDS_QKV = GQKV::LDS;
  constexpr int LDS_SC = GSC::LDS;
  constexpr int LDS_PV = GPV::LDS + 64 * 8 * 4;
  constexpr int LDS_WO = GPV::LDS;
  constexpr int LDS_FC = GFC::LDS;
  static_assert(LDS_QKV <= 163840 && LDS_SC <= 163840 && LDS_PV <= 163840 && LDS_FC <= 163840, "lds");
  (void)hipFuncSetAttribute((const void*)k_qkv, hipFuncAttributeMaxDynamicSharedMemorySize, LDS_QKV);
  (void)hipFuncSetAttribute((const void*)k_scores, hipFuncAttributeMaxDynamicSharedMemorySize, LDS_SC);
  (void)hipFuncSetAttribute((const void*)k_pv, hipFuncAttributeMaxDynamicSharedMemorySize, LDS_PV);
  (void)hipFuncSetAttribute((const void*)k_wo, hipFuncAttributeMaxDynamicSharedMemorySize, LDS_WO);
  (void)hipFuncSetAttribute((const void*)k_fc1, hipFuncAttributeMaxDynamicSharedMemorySize, LDS_FC);
  (void)hipFuncSetAttribute((const void*)k_fc2, hipFuncAttributeMaxDynamicSharedMemorySize, LDS_FC);

  k_mod<<<144, 256, 0, stream>>>(c, wmod, mod);
  k_lnmod<<<MROWS / 4, 256, 0, stream>>>(x, mod, 0, h);
  k_qkv<<<256, 512, LDS_QKV, stream>>>(h, wqkv, qkv);
  k_scores<<<256, 512, LDS_SC, stream>>>(qkv, E, st);
  k_pv<<<256, 512, LDS_PV, stream>>>(E, st, qkv, O);
  k_wo<<<256, 512, LDS_WO, stream>>>(O, wo, x, mod, x1);
  k_lnmod<<<MROWS / 4, 256, 0, stream>>>(x1, mod, 3, h2);
  k_fc1<<<256, 512, LDS_FC, stream>>>(h2, w1, f);
  k_fc2<<<256, 512, LDS_FC, stream>>>(f, w2, part);
  k_final<<<MROWS * DM / 8 / 256, 256, 0, stream>>>(part, x1, mod, out);
}
```

```cpp
#include <hip/hip_runtime.h>
#include <hip/hip_bf16.h>
#include <stdint.h>

typedef unsigned short u16;
typedef __attribute__((ext_vector_type(8))) __bf16 bf16x8;
typedef __attribute__((ext_vector_type(4))) __bf16 bf16x4;
typedef __attribute__((ext_vector_type(2))) __bf16 bf16x2;
typedef __attribute__((ext_vector_type(4))) float f32x4;
typedef __attribute__((ext_vector_type(2))) float f32x2;
typedef __attribute__((ext_vector_type(4))) unsigned u32x4;
typedef __attribute__((ext_vector_type(2))) unsigned u32x2;
#define LAS __attribute__((address_space(3)))
#define GAS __attribute__((address_space(1)))

constexpr int NB = 2, NSEQ = 1024, DM = 768, NH = 2, DH = 384, FF = 3072;
constexpr int MROWS = NB * NSEQ;

__device__ __forceinline__ float bf2f(u16 v) { return __uint_as_float(((unsigned)v) << 16); }
__device__ __forceinline__ float bflo(unsigned v) { return __uint_as_float(v << 16); }
__device__ __forceinline__ float bfhi(unsigned v) { return __uint_as_float(v & 0xffff0000u); }
__device__ __forceinline__ unsigned pack2(float a, float b) {
  f32x2 f = {a, b};
  bf16x2 r = __builtin_convertvector(f, bf16x2);
  return __builtin_bit_cast(unsigned, r);
}
__device__ __forceinline__ float rbf(float a) {
  return bflo(pack2(a, 0.f) & 0xffffu);
}

template <int N>
__device__ __forceinline__ void waitv() {
  asm volatile("s_waitcnt vmcnt(%0)" ::"i"(N) : "memory");
}
template <int I>
struct IC {
  static constexpr int value = I;
};
template <int B, int E, class F>
__device__ __forceinline__ void static_for(F&& f) {
  if constexpr (B < E) {
    f(IC<B>{});
    static_for<B + 1, E>(f);
  }
}
#define FENCE() asm volatile("" ::: "memory")
#define STAMP(kid, i)
#define BARRIER()                        \
  do {                                   \
    FENCE();                             \
    __builtin_amdgcn_s_barrier();        \
    FENCE();                             \
  } while (0)

template <int BM, int BN, int WM, int WN, int NS, bool BKN>
struct GC {
  static constexpr int NW = WM * WN;
  static constexpr int WTM = BM / WM, MT = WTM / 16;
  static constexpr int NTT = BN / 16;
  static constexpr int NT = (NTT + WN - 1) / WN;
  static constexpr int WTN = NT * 16;
  static constexpr int A_BYTES = BM * 128, B_BYTES = BN * 128, STAGE = A_BYTES + B_BYTES;
  static constexpr int NA = BM / 8, NBI = BN / 8;
  static constexpr int LA = NA / 4;
  static constexpr int LB = (NBI + 3) / 4;
  static constexpr int NFULL = NBI % 4 == 0 ? 4 : NBI % 4;
  static constexpr int L = LA + LB;
  static constexpr int LDS = NS * STAGE;
  static_assert(NW == 8, "8 waves");
  static_assert(NA % 4 == 0, "A instr split");
  static_assert(NS >= 4, "stages");
};

template <int BM, int BN, int WM, int WN, int NS, bool BKN, int T, int KID, class Hook>
__device__ __forceinline__ void gemm_main(const u16* __restrict__ Ab, int lda, const u16* __restrict__ Bb, int ldb,
                                          char* smem, f32x4 (&acc)[GC<BM, BN, WM, WN, NS, BKN>::MT][GC<BM, BN, WM, WN, NS, BKN>::NT],
                                          Hook hook) {
  using C = GC<BM, BN, WM, WN, NS, BKN>;
  constexpr int MT = C::MT, NT = C::NT, L = C::L, LA = C::LA, LB = C::LB;
  const int tid = threadIdx.x, lane = tid & 63;
  const int wave = __builtin_amdgcn_readfirstlane(tid >> 6);
  const int wm = wave % WM, wn = wave / WM;
  const int wq = wave & 3, par = wave >> 2;
  const bool full = (C::NFULL == 4) || (wq < C::NFULL);

  unsigned goff[L];
#pragma unroll
  for (int i = 0; i < LA; ++i) {
    int r = i * 4 + wq;
    int row = r * 8 + (lane >> 3);
    int lc = (lane & 7) ^ (row & 7);
    goff[i] = (unsigned)(row * lda + lc * 8) * 2u;
  }
#pragma unroll
  for (int i = 0; i < LB; ++i) {
    int r = i * 4 + wq;
    if (r >= C::NBI) r = 0;
    if (BKN) {
      int st = r * 4 + (lane >> 4);
      int kb = st / (BN / 16), nb = st % (BN / 16);
      int i16 = lane & 15, prow = i16 >> 1, hf = i16 & 1;
      int lrow = prow ^ ((kb & 1) << 2);
      goff[LA + i] = (unsigned)((kb * 8 + lrow) * ldb + nb * 16 + hf * 8) * 2u;
    } else {
      int row = r * 8 + (lane >> 3);
      int lc = (lane & 7) ^ (row & 7);
      goff[LA + i] = (unsigned)(row * ldb + lc * 8) * 2u;
    }
  }
  const char* Abase = (const char*)Ab;
  const char* Bbase = (const char*)Bb;
  const size_t bstep = BKN ? (size_t)64 * ldb * 2 : (size_t)128;

  auto issue1 = [&](auto TT, auto II) {
    constexpr int t = TT.value, i = II.value;
    char* sb = smem + (t % NS) * C::STAGE + wq * 1024;
    if constexpr (i < LA) {
      __builtin_amdgcn_global_load_lds((const GAS unsigned*)(Abase + (size_t)t * 128 + goff[i]),
                                       (LAS unsigned*)(sb + i * 4096), 16, 0, 0);
    } else {
      if (i + 1 < L || full)
        __builtin_amdgcn_global_load_lds((const GAS unsigned*)(Bbase + (size_t)t * bstep + goff[i]),
                                         (LAS unsigned*)(sb + C::A_BYTES + (i - LA) * 4096), 16, 0, 0);
    }
  };
  auto issue_all = [&](auto TT) { static_for<0, L>([&](auto II) { issue1(TT, II); }); };
  auto wait_st = [&](auto NN) {
    constexpr int n = NN.value;
    if (C::NFULL == 4 || n == 0) {
      waitv<n * L>();
    } else {
      if (full) waitv<n * L>();
      else waitv<n * (L - 1)>();
    }
  };

  const int g = lane >> 4, i16 = lane & 15;
  unsigned aoff[2], boff[2];
#pragma unroll
  for (int s = 0; s < 2; ++s)
    aoff[s] = (unsigned)((wm * C::WTM + i16) * 128 + (((s * 4 + g) ^ (lane & 7)) << 4));
  if (BKN) {
    const int q4 = i16 >> 2, p = i16 & 3;
#pragma unroll
    for (int h = 0; h < 2; ++h)
      boff[h] = (unsigned)(C::A_BYTES + (g * (BN / 16) + wn * NT) * 256 + (((h * 4 + q4) ^ ((g & 1) << 2)) * 32) +
                           p * 8);
  } else {
#pragma unroll
    for (int s = 0; s < 2; ++s)
      boff[s] = (unsigned)(C::A_BYTES + (wn * C::WTN + i16) * 128 + (((s * 4 + g) ^ (lane & 7)) << 4));
  }
  const bool last_dummy = (C::NTT % WN != 0) && (wn * NT + NT > C::NTT);
  const int last_n = last_dummy ? 0 : NT - 1;

  auto rd_frag = [&](int slot, auto SS, bf16x8 (&fa)[MT], bf16x8 (&fb)[NT]) {
    constexpr int s = SS.value;
    const char* sb = smem + slot * C::STAGE;
#pragma unroll
    for (int m = 0; m < MT; ++m) fa[m] = *(const bf16x8*)(sb + aoff[s] + m * 2048);
#pragma unroll
    for (int n = 0; n < NT; ++n) {
      const int nn = (n == NT - 1) ? last_n : n;
      if (BKN) {
        bf16x4 lo = __builtin_amdgcn_ds_read_tr16_b64_v4bf16(
            (LAS bf16x4*)(sb + boff[0] + s * 4 * (BN / 16) * 256 + nn * 256));
        bf16x4 hi = __builtin_amdgcn_ds_read_tr16_b64_v4bf16(
            (LAS bf16x4*)(sb + boff[1] + s * 4 * (BN / 16) * 256 + nn * 256));
        fb[n] = __builtin_shufflevector(lo, hi, 0, 1, 2, 3, 4, 5, 6, 7);
      } else {
        fb[n] = *(const bf16x8*)(sb + boff[s] + nn * 2048);
      }
    }
  };
  auto mma = [&](bf16x8 (&fa)[MT], bf16x8 (&fb)[NT], auto TI, bool do_issue) {
    constexpr int ti = TI.value;
    static_for<0, MT * NT>([&](auto Q) {
      constexpr int q = Q.value, m = q / NT, n = q % NT;
      acc[m][n] = __builtin_amdgcn_mfma_f32_16x16x32_bf16(fb[n], fa[m], acc[m][n], 0, 0, 0);
      if constexpr (ti >= 0) {
        constexpr int lo = (q * L) / (MT * NT), hi = ((q + 1) * L) / (MT * NT);
        if constexpr (hi > lo) {
          if (do_issue) static_for<lo, hi>([&](auto II) { issue1(IC<ti>{}, II); });
          __builtin_amdgcn_sched_barrier(0);
        }
      }
    });
  };

  STAMP(KID, 0);
  static_for<0, NS - 1>([&](auto S) {
    if ((S.value & 1) == par) issue_all(S);
  });
  if (par == 0) wait_st(IC<(NS - 2) / 2>{});
  BARRIER();
  STAMP(KID, 1);
  bf16x8 fa0[MT], fb0[NT], fa1[MT], fb1[NT];
  rd_frag(0, IC<0>{}, fa0, fb0);
  static_for<0, T>([&](auto TT) {
    constexpr int t = TT.value;
    rd_frag(t % NS, IC<1>{}, fa1, fb1);
    __builtin_amdgcn_sched_barrier(0);
    mma(fa0, fb0, IC<-1>{}, false);
    __builtin_amdgcn_sched_barrier(0);
    if constexpr (t + 1 < T) {
      constexpr int last_issued = (t + NS - 2) < (T - 1) ? (t + NS - 2) : (T - 1);
      constexpr int younger = last_issued >= t + 3 ? (last_issued - (t + 1)) / 2 : 0;
      if (par == ((t + 1) & 1)) wait_st(IC<younger>{});
      BARRIER();
      rd_frag((t + 1) % NS, IC<0>{}, fa0, fb0);
      __builtin_amdgcn_sched_barrier(0);
    }
    if constexpr (t + 1 < T && t + NS - 1 < T) mma(fa1, fb1, IC<t + NS - 1>{}, par == ((t + NS - 1) & 1));
    else mma(fa1, fb1, IC<-1>{}, false);
    hook(TT);
    __builtin_amdgcn_sched_barrier(0);
  });
  STAMP(KID, 2);
}

struct NoHook {
  template <class X>
  __device__ __forceinline__ void operator()(X) const {}
};

template <int CTRL>
__device__ __forceinline__ float dppf(float x) {
  return __builtin_bit_cast(float, __builtin_amdgcn_update_dpp(0, __builtin_bit_cast(int, x), CTRL, 0xf, 0xf, true));
}
__device__ __forceinline__ float red16_max(float x) {
  x = fmaxf(x, dppf<0xB1>(x));
  x = fmaxf(x, dppf<0x4E>(x));
  x = fmaxf(x, dppf<0x124>(x));
  x = fmaxf(x, dppf<0x128>(x));
  return x;
}
__device__ __forceinline__ float red16_sum(float x) {
  x += dppf<0xB1>(x);
  x += dppf<0x4E>(x);
  x += dppf<0x124>(x);
  x += dppf<0x128>(x);
  return x;
}

__global__ __launch_bounds__(256) void k_mod(const u16* __restrict__ c, const u16* __restrict__ wmod,
                                             float* __restrict__ mod) {
  __shared__ float red[2][64][33];
  const int tid = threadIdx.x, cg = tid & 3, kq = tid >> 2;
  const int col0 = blockIdx.x * 32 + cg * 8;
  u32x4 w[12];
#pragma unroll
  for (int i = 0; i < 12; ++i) {
    int k = i * 64 + kq;
    w[i] = *(const u32x4*)(wmod + (size_t)k * (6 * DM) + col0);
  }
  float a0[8], a1[8];
#pragma unroll
  for (int j = 0; j < 8; ++j) a0[j] = a1[j] = 0.f;
#pragma unroll
  for (int i = 0; i < 12; ++i) {
    int k = i * 64 + kq;
    float c0 = bf2f(c[k]), c1 = bf2f(c[DM + k]);
    float s0 = rbf(c0 / (1.f + __expf(-c0)));
    float s1 = rbf(c1 / (1.f + __expf(-c1)));
#pragma unroll
    for (int j = 0; j < 4; ++j) {
      float wl = bflo(w[i][j]), wh = bfhi(w[i][j]);
      a0[2 * j] += s0 * wl; a0[2 * j + 1] += s0 * wh;
      a1[2 * j] += s1 * wl; a1[2 * j + 1] += s1 * wh;
    }
  }
#pragma unroll
  for (int j = 0; j < 8; ++j) {
    red[0][kq][cg * 8 + j] = a0[j];
    red[1][kq][cg * 8 + j] = a1[j];
  }
  __syncthreads();
  if (tid < 64) {
    int b = tid >> 5, col = tid & 31;
    float s = 0.f;
    for (int q = 0; q < 64; ++q) s += red[b][q][col];
    mod[b * (6 * DM) + blockIdx.x * 32 + col] = rbf(s);
  }
}

__global__ __launch_bounds__(256) void k_lnmod(const u16* __restrict__ x, const float* __restrict__ mod,
                                               int shift_idx, u16* __restrict__ out) {
  const int lane = threadIdx.x & 63, row = blockIdx.x * 4 + (threadIdx.x >> 6);
  const int b = row >> 10;
  const u16* xr = x + (size_t)row * DM;
  float v[12];
#pragma unroll
  for (int i = 0; i < 3; ++i) {
    u32x2 u = *(const u32x2*)(xr + i * 256 + lane * 4);
    v[i * 4 + 0] = bflo(u[0]); v[i * 4 + 1] = bfhi(u[0]);
    v[i * 4 + 2] = bflo(u[1]); v[i * 4 + 3] = bfhi(u[1]);
  }
  float s = 0.f;
#pragma unroll
  for (int i = 0; i < 12; ++i) s += v[i];
#pragma unroll
  for (int o = 32; o; o >>= 1) s += __shfl_xor(s, o);
  const float mean = s * (1.f / DM);
  float q = 0.f;
#pragma unroll
  for (int i = 0; i < 12; ++i) { float d = v[i] - mean; q += d * d; }
#pragma unroll
  for (int o = 32; o; o >>= 1) q += __shfl_xor(q, o);
  const float mb = rbf(mean), vb = rbf(q * (1.f / DM));
  const float sd = rbf(__fsqrt_rn(rbf(vb + 1e-5f)));
  const float* shift = mod + b * (6 * DM) + shift_idx * DM;
  const float* scale = shift + DM;
#pragma unroll
  for (int i = 0; i < 3; ++i) {
    int c0 = i * 256 + lane * 4;
    f32x4 sc = *(const f32x4*)(scale + c0), sh = *(const f32x4*)(shift + c0);
    float h[4];
#pragma unroll
    for (int j = 0; j < 4; ++j) {
      float n = rbf(rbf(v[i * 4 + j] - mb) / sd);
      h[j] = rbf(n * rbf(1.f + sc[j])) + sh[j];
    }
    u32x2 o = {pack2(h[0], h[1]), pack2(h[2], h[3])};
    *(u32x2*)(out + (size_t)row * DM + c0) = o;
  }
}

using GQKV = GC<128, 144, 4, 2, 4, true>;
__global__ __launch_bounds__(512, 2) void k_qkv(const u16* __restrict__ h, const u16* __restrict__ w,
                                                u16* __restrict__ qkv) {
  extern __shared__ __attribute__((aligned(16))) char smem[];
  using C = GQKV;
  constexpr int BM = 128, BN = 144;
  const int x = blockIdx.x & 7, j = blockIdx.x >> 3;
  const int tm = (x >> 2) * 8 + (j >> 2), tn = (x & 3) * 4 + (j & 3);
  const int m0 = tm * BM, n0 = tn * BN;
  f32x4 acc[C::MT][C::NT] = {};
  gemm_main<BM, BN, 4, 2, 4, true, 12, 0>(h + (size_t)m0 * DM, DM, w + n0, 3 * DM, smem, acc, NoHook());
  const int lane = threadIdx.x & 63, wave = threadIdx.x >> 6, wm = wave % 4, wn = wave / 4;
#pragma unroll
  for (int m = 0; m < C::MT; ++m)
#pragma unroll
    for (int n = 0; n < C::NT; ++n) {
      if (wn * C::NT + n < C::NTT) {
        int row = m0 + wm * C::WTM + m * 16 + (lane & 15);
        int col = n0 + wn * C::WTN + n * 16 + (lane >> 4) * 4;
        u32x2 v = {pack2(acc[m][n][0], acc[m][n][1]), pack2(acc[m][n][2], acc[m][n][3])};
        *(u32x2*)(qkv + (size_t)row * (3 * DM) + col) = v;
      }
    }
  STAMP(0, 3);
}

using GSC = GC<128, 128, 4, 2, 4, false>;
__global__ __launch_bounds__(512, 2) void k_scores(const u16* __restrict__ qkv, u16* __restrict__ E,
                                                   f32x2* __restrict__ st) {
  extern __shared__ __attribute__((aligned(16))) char smem[];
  using C = GSC;
  constexpr int BM = 128, BN = 128;
  const int x = blockIdx.x & 7, j = blockIdx.x >> 3;
  const int bh = x >> 1, b = bh >> 1, hh = bh & 1;
  const int tm = (x & 1) * 4 + (j >> 3), tn = j & 7;
  const int m0 = tm * BM, n0 = tn * BN;
  const u16* Q = qkv + (size_t)(b * NSEQ + m0) * (3 * DM) + hh * DH;
  const u16* K = qkv + (size_t)(b * NSEQ + n0) * (3 * DM) + DM + hh * DH;
  f32x4 acc[C::MT][C::NT] = {};
  gemm_main<BM, BN, 4, 2, 4, false, 6, 1>(Q, 3 * DM, K, 3 * DM, smem, acc, NoHook());
  const int lane = threadIdx.x & 63, wave = threadIdx.x >> 6, wm = wave % 4, wn = wave / 4;
  const float scale = 0.05103103630798288f;
#pragma unroll
  for (int m = 0; m < C::MT; ++m) {
    float s[C::NT][4];
    float mx = -INFINITY;
#pragma unroll
    for (int n = 0; n < C::NT; ++n) {
      unsigned p0 = pack2(acc[m][n][0], acc[m][n][1]), p1 = pack2(acc[m][n][2], acc[m][n][3]);
      p0 = pack2(bflo(p0) * scale, bfhi(p0) * scale);
      p1 = pack2(bflo(p1) * scale, bfhi(p1) * scale);
      s[n][0] = bflo(p0); s[n][1] = bfhi(p0); s[n][2] = bflo(p1); s[n][3] = bfhi(p1);
      mx = fmaxf(mx, fmaxf(fmaxf(s[n][0], s[n][1]), fmaxf(s[n][2], s[n][3])));
    }
    mx = fmaxf(mx, __shfl_xor(mx, 16));
    mx = fmaxf(mx, __shfl_xor(mx, 32));
    float sum = 0.f;
    const int row = m0 + wm * C::WTM + m * 16 + (lane & 15);
    u16* Er = E + ((size_t)bh * NSEQ + row) * NSEQ + n0 + wn * C::WTN + (lane >> 4) * 4;
#pragma unroll
    for (int n = 0; n < C::NT; ++n) {
#pragma unroll
      for (int e = 0; e < 4; ++e) {
        s[n][e] = __expf(s[n][e] - mx);
        sum += s[n][e];
      }
      u32x2 v = {pack2(s[n][0], s[n][1]), pack2(s[n][2], s[n][3])};
      *(u32x2*)(Er + n * 16) = v;
    }
    sum += __shfl_xor(sum, 16);
    sum += __shfl_xor(sum, 32);
    if (lane < 16) st[((size_t)bh * NSEQ + row) * 16 + tn * 2 + wn] = f32x2{mx, sum};
  }
  STAMP(1, 3);
}

using GPV = GC<64, 96, 4, 2, 6, true>;
__global__ __launch_bounds__(512, 2) void k_pv(const u16* __restrict__ E, const f32x2* __restrict__ st,
                                               const u16* __restrict__ qkv, u16* __restrict__ O) {
  extern __shared__ __attribute__((aligned(16))) char smem[];
  using C = GPV;
  constexpr int BM = 64, BN = 96;
  const int x = blockIdx.x & 7, j = blockIdx.x >> 3;
  const int bh = x >> 1, b = bh >> 1, hh = bh & 1;
  const int tm = (x & 1) * 8 + (j >> 2), tn = j & 3;
  const int m0 = tm * BM, n0 = tn * BN;
  const int lane = threadIdx.x & 63, wave = threadIdx.x >> 6, wm = wave % 4, wn = wave / 4;
  float fac[16];
  {
    const f32x4* p = (const f32x4*)(st + ((size_t)bh * NSEQ + m0 + wm * 16 + (lane & 15)) * 16);
    f32x4 v[8];
#pragma unroll
    for (int t = 0; t < 8; ++t) v[t] = p[t];
    float M = -INFINITY;
#pragma unroll
    for (int t = 0; t < 8; ++t) M = fmaxf(M, fmaxf(v[t][0], v[t][2]));
    float Ls = 0.f;
#pragma unroll
    for (int t = 0; t < 8; ++t) {
      fac[2 * t] = __expf(v[t][0] - M);
      fac[2 * t + 1] = __expf(v[t][2] - M);
      Ls += fac[2 * t] * v[t][1] + fac[2 * t + 1] * v[t][3];
    }
    float inv = 1.f / Ls;
#pragma unroll
    for (int t = 0; t < 16; ++t) fac[t] *= inv;
  }
  const u16* A = E + ((size_t)bh * NSEQ + m0) * NSEQ;
  const u16* V = qkv + (size_t)(b * NSEQ) * (3 * DM) + 2 * DM + hh * DH + n0;
  f32x4 acc[1][3] = {};
  f32x4 tot[1][3] = {};
  auto hook = [&](auto TT) {
    constexpr int t = TT.value;
#pragma unroll
    for (int n = 0; n < 3; ++n) {
      tot[0][n] += acc[0][n] * fac[t];
      acc[0][n] = f32x4{0.f, 0.f, 0.f, 0.f};
    }
  };
  gemm_main<BM, BN, 4, 2, 6, true, 16, 2>(A, NSEQ, V, 3 * DM, smem, acc, hook);
#pragma unroll
  for (int n = 0; n < 3; ++n) {
    int row = b * NSEQ + m0 + wm * 16 + (lane & 15);
    int col = hh * DH + n0 + wn * C::WTN + n * 16 + (lane >> 4) * 4;
    u32x2 v = {pack2(tot[0][n][0], tot[0][n][1]), pack2(tot[0][n][2], tot[0][n][3])};
    *(u32x2*)(O + (size_t)row * DM + col) = v;
  }
  STAMP(2, 3);
}

__global__ __launch_bounds__(512, 2) void k_wo(const u16* __restrict__ O, const u16* __restrict__ wo,
                                               const u16* __restrict__ xin, const float* __restrict__ mod,
                                               u16* __restrict__ x1) {
  extern __shared__ __attribute__((aligned(16))) char smem[];
  using C = GPV;
  constexpr int BM = 64, BN = 96;
  const int x = blockIdx.x & 7, j = blockIdx.x >> 3;
  const int tm = (x >> 1) * 8 + (j >> 2), tn = (x & 1) * 4 + (j & 3);
  const int m0 = tm * BM, n0 = tn * BN, b = m0 >> 10;
  const int lane = threadIdx.x & 63, wave = threadIdx.x >> 6, wm = wave % 4, wn = wave / 4;
  const int row = m0 + wm * 16 + (lane & 15);
  const int col0 = n0 + wn * C::WTN + (lane >> 4) * 4;
  u32x2 xv[3];
  f32x4 gt[3];
#pragma unroll
  for (int n = 0; n < 3; ++n) {
    xv[n] = *(const u32x2*)(xin + (size_t)row * DM + col0 + n * 16);
    gt[n] = *(const f32x4*)(mod + b * (6 * DM) + 2 * DM + col0 + n * 16);
  }
  f32x4 acc[1][3] = {};
  gemm_main<BM, BN, 4, 2, 6, true, 12, 3>(O + (size_t)m0 * DM, DM, wo + n0, DM, smem, acc, NoHook());
#pragma unroll
  for (int n = 0; n < 3; ++n) {
    unsigned a0 = pack2(acc[0][n][0], acc[0][n][1]), a1 = pack2(acc[0][n][2], acc[0][n][3]);
    unsigned g0 = pack2(gt[n][0] * bflo(a0), gt[n][1] * bfhi(a0));
    unsigned g1 = pack2(gt[n][2] * bflo(a1), gt[n][3] * bfhi(a1));
    u32x2 o = {pack2(bflo(xv[n][0]) + bflo(g0), bfhi(xv[n][0]) + bfhi(g0)),
               pack2(bflo(xv[n][1]) + bflo(g1), bfhi(xv[n][1]) + bfhi(g1))};
    *(u32x2*)(x1 + (size_t)row * DM + col0 + n * 16) = o;
  }
  STAMP(3, 3);
}

__device__ __forceinline__ float gelu_tanh(float u) {
  float w = u * (-2.f * 0.7978845608028654f * 1.4426950408889634f);
  float a = w * (1.f + 0.044715f * u * u);
  return u * __builtin_amdgcn_rcpf(1.f + __builtin_amdgcn_exp2f(a));
}
using GFC = GC<128, 192, 2, 4, 4, true>;
__global__ __launch_bounds__(512, 2) void k_fc1(const u16* __restrict__ h2, const u16* __restrict__ w1,
                                                u16* __restrict__ f) {
  extern __shared__ __attribute__((aligned(16))) char smem[];
  using C = GFC;
  constexpr int BM = 128, BN = 192;
  const int x = blockIdx.x & 7, j = blockIdx.x >> 3;
  const int tm = (x >> 2) * 8 + (j >> 2), tn = (x & 3) * 4 + (j & 3);
  const int m0 = tm * BM, n0 = tn * BN;
  f32x4 acc[C::MT][C::NT] = {};
  gemm_main<BM, BN, 2, 4, 4, true, 12, 4>(h2 + (size_t)m0 * DM, DM, w1 + n0, FF, smem, acc, NoHook());
  const int lane = threadIdx.x & 63, wave = threadIdx.x >> 6, wm = wave % 2, wn = wave / 2;
#pragma unroll
  for (int m = 0; m < C::MT; ++m)
#pragma unroll
    for (int n = 0; n < C::NT; ++n) {
      int row = m0 + wm * C::WTM + m * 16 + (lane & 15);
      int col = n0 + wn * C::WTN + n * 16 + (lane >> 4) * 4;
      unsigned p0 = pack2(acc[m][n][0], acc[m][n][1]), p1 = pack2(acc[m][n][2], acc[m][n][3]);
      u32x2 v = {pack2(gelu_tanh(bflo(p0)), gelu_tanh(bfhi(p0))), pack2(gelu_tanh(bflo(p1)), gelu_tanh(bfhi(p1)))};
      *(u32x2*)(f + (size_t)row * FF + col) = v;
    }
  STAMP(4, 3);
}

__global__ __launch_bounds__(512, 2) void k_fc2(const u16* __restrict__ f, const u16* __restrict__ w2,
                                                float* __restrict__ part) {
  extern __shared__ __attribute__((aligned(16))) char smem[];
  using C = GFC;
  constexpr int BM = 128, BN = 192;
  const int x = blockIdx.x & 7, j = blockIdx.x >> 3;
  const int ks = x >> 1, tm = (x & 1) * 8 + (j >> 2), tn = j & 3;
  const int m0 = tm * BM, n0 = tn * BN;
  f32x4 acc[C::MT][C::NT] = {};
  gemm_main<BM, BN, 2, 4, 4, true, 12, 5>(f + (size_t)m0 * FF + ks * 768, FF, w2 + (size_t)ks * 768 * DM + n0, DM,
                                          smem, acc, NoHook());
  const int lane = threadIdx.x & 63, wave = threadIdx.x >> 6, wm = wave % 2, wn = wave / 2;
  float* P = part + (size_t)ks * MROWS * DM;
#pragma unroll
  for (int m = 0; m < C::MT; ++m)
#pragma unroll
    for (int n = 0; n < C::NT; ++n) {
      int row = m0 + wm * C::WTM + m * 16 + (lane & 15);
      int col = n0 + wn * C::WTN + n * 16 + (lane >> 4) * 4;
      *(f32x4*)(P + (size_t)row * DM + col) = acc[m][n];
    }
  STAMP(5, 3);
}

__global__ __launch_bounds__(256) void k_final(const float* __restrict__ part, const u16* __restrict__ x1,
                                               const float* __restrict__ mod, u16* __restrict__ out) {
  const int idx = blockIdx.x * 256 + threadIdx.x;
  const int row = idx / (DM / 8), c8 = idx % (DM / 8), b = row >> 10;
  const size_t off = (size_t)row * DM + c8 * 8;
  f32x4 s0 = {0.f, 0.f, 0.f, 0.f}, s1 = s0;
#pragma unroll
  for (int k = 0; k < 4; ++k) {
    s0 += *(const f32x4*)(part + (size_t)k * MROWS * DM + off);
    s1 += *(const f32x4*)(part + (size_t)k * MROWS * DM + off + 4);
  }
  const float* gate = mod + b * (6 * DM) + 5 * DM + c8 * 8;
  f32x4 g0 = *(const f32x4*)gate, g1 = *(const f32x4*)(gate + 4);
  u32x4 xv = *(const u32x4*)(x1 + off);
  u32x4 o;
#pragma unroll
  for (int e = 0; e < 4; ++e) {
    float ma = e < 2 ? s0[2 * e] : s1[2 * e - 4], mb = e < 2 ? s0[2 * e + 1] : s1[2 * e - 3];
    float ga = e < 2 ? g0[2 * e] : g1[2 * e - 4], gb = e < 2 ? g0[2 * e + 1] : g1[2 * e - 3];
    float r0 = bflo(xv[e]) + rbf(ga * rbf(ma));
    float r1 = bfhi(xv[e]) + rbf(gb * rbf(mb));
    o[e] = pack2(r0, r1);
  }
  *(u32x4*)(out + off) = o;
}

extern "C" void kernel_launch(void* const* d_in, const int* in_sizes, int n_in, void* d_out, int out_size,
                              void* d_ws, size_t ws_size, hipStream_t stream) {
  const u16* x = (const u16*)d_in[0];
  const u16* c = (const u16*)d_in[1];
  const u16* wqkv = (const u16*)d_in[2];
  const u16* wo = (const u16*)d_in[3];
  const u16* wmod = (const u16*)d_in[4];
  const u16* w1 = (const u16*)d_in[5];
  const u16* w2 = (const u16*)d_in[6];
  u16* out = (u16*)d_out;

  char* ws = (char*)d_ws;
  size_t o = 0;
  auto take = [&](size_t bytes) { char* p = ws + o; o += (bytes + 255) & ~(size_t)255; return p; };
  float* mod = (float*)take(2 * 6 * DM * 4);
  u16* h = (u16*)take((size_t)MROWS * DM * 2);
  u16* qkv = (u16*)take((size_t)MROWS * 3 * DM * 2);
  u16* E = (u16*)take((size_t)4 * NSEQ * NSEQ * 2);
  f32x2* st = (f32x2*)take((size_t)4 * NSEQ * 16 * 8);
  u16* O = (u16*)take((size_t)MROWS * DM * 2);
  u16* x1 = (u16*)take((size_t)MROWS * DM * 2);
  u16* h2 = (u16*)take((size_t)MROWS * DM * 2);
  u16* f = (u16*)take((size_t)MROWS * FF * 2);
  float* part = (float*)take((size_t)4 * MROWS * DM * 4);

  constexpr int LDS_QKV = GQKV::LDS;
  constexpr int LDS_SC = GSC::LDS;
  constexpr int LDS_PV = GPV::LDS;
  constexpr int LDS_WO = GPV::LDS;
  constexpr int LDS_FC = GFC::LDS;
  static_assert(LDS_QKV <= 163840 && LDS_SC <= 163840 && LDS_PV <= 163840 && LDS_FC <= 163840, "lds");
  (void)hipFuncSetAttribute((const void*)k_qkv, hipFuncAttributeMaxDynamicSharedMemorySize, LDS_QKV);
  (void)hipFuncSetAttribute((const void*)k_scores, hipFuncAttributeMaxDynamicSharedMemorySize, LDS_SC);
  (void)hipFuncSetAttribute((const void*)k_pv, hipFuncAttributeMaxDynamicSharedMemorySize, LDS_PV);
  (void)hipFuncSetAttribute((const void*)k_wo, hipFuncAttributeMaxDynamicSharedMemorySize, LDS_WO);
  (void)hipFuncSetAttribute((const void*)k_fc1, hipFuncAttributeMaxDynamicSharedMemorySize, LDS_FC);
  (void)hipFuncSetAttribute((const void*)k_fc2, hipFuncAttributeMaxDynamicSharedMemorySize, LDS_FC);

  k_mod<<<144, 256, 0, stream>>>(c, wmod, mod);
  k_lnmod<<<MROWS / 4, 256, 0, stream>>>(x, mod, 0, h);
  k_qkv<<<256, 512, LDS_QKV, stream>>>(h, wqkv, qkv);
  k_scores<<<256, 512, LDS_SC, stream>>>(qkv, E, st);
  k_pv<<<256, 512, LDS_PV, stream>>>(E, st, qkv, O);
  k_wo<<<256, 512, LDS_WO, stream>>>(O, wo, x, mod, x1);
  k_lnmod<<<MROWS / 4, 256, 0, stream>>>(x1, mod, 3, h2);
  k_fc1<<<256, 512, LDS_FC, stream>>>(h2, w1, f);
  k_fc2<<<256, 512, LDS_FC, stream>>>(f, w2, part);
  k_final<<<MROWS * DM / 8 / 256, 256, 0, stream>>>(part, x1, mod, out);
}
```

```cpp
#include <hip/hip_runtime.h>
#include <hip/hip_bf16.h>
#include <stdint.h>

typedef unsigned short u16;
typedef __attribute__((ext_vector_type(8))) __bf16 bf16x8;
typedef __attribute__((ext_vector_type(4))) __bf16 bf16x4;
typedef __attribute__((ext_vector_type(2))) __bf16 bf16x2;
typedef __attribute__((ext_vector_type(4))) float f32x4;
typedef __attribute__((ext_vector_type(2))) float f32x2;
typedef __attribute__((ext_vector_type(4))) unsigned u32x4;
typedef __attribute__((ext_vector_type(2))) unsigned u32x2;
#define LAS __attribute__((address_space(3)))
#define GAS __attribute__((address_space(1)))

constexpr int NB = 2, NSEQ = 1024, DM = 768, NH = 2, DH = 384, FF = 3072;
constexpr int MROWS = NB * NSEQ;

__device__ __forceinline__ float bf2f(u16 v) { return __uint_as_float(((unsigned)v) << 16); }
__device__ __forceinline__ float bflo(unsigned v) { return __uint_as_float(v << 16); }
__device__ __forceinline__ float bfhi(unsigned v) { return __uint_as_float(v & 0xffff0000u); }
__device__ __forceinline__ unsigned pack2(float a, float b) {
  f32x2 f = {a, b};
  bf16x2 r = __builtin_convertvector(f, bf16x2);
  return __builtin_bit_cast(unsigned, r);
}
__device__ __forceinline__ float rbf(float a) {
  return bflo(pack2(a, 0.f) & 0xffffu);
}

template <int N>
__device__ __forceinline__ void waitv() {
  asm volatile("s_waitcnt vmcnt(%0)" ::"i"(N) : "memory");
}
template <int I>
struct IC {
  static constexpr int value = I;
};
template <int B, int E, class F>
__device__ __forceinline__ void static_for(F&& f) {
  if constexpr (B < E) {
    f(IC<B>{});
    static_for<B + 1, E>(f);
  }
}
#define ABL 0
#define FENCE() asm volatile("" ::: "memory")
#define STAMP(kid, i)
#define BARRIER()                        \
  do {                                   \
    FENCE();                             \
    __builtin_amdgcn_s_barrier();        \
    FENCE();                             \
  } while (0)

template <int BM, int BN, int WM, int WN, int NS, bool BKN, bool ATR>
struct GC {
  static constexpr int NW = WM * WN;
  static constexpr int WTM = BM / WM, MT = WTM / 16;
  static constexpr int NTT = BN / 16;
  static constexpr int NT = (NTT + WN - 1) / WN;
  static constexpr int WTN = NT * 16;
  static constexpr int A_BYTES = BM * 128, B_BYTES = BN * 128;
  static constexpr int NSA = ATR ? 3 : NS;
  static constexpr int B_OFF = NSA * A_BYTES;
  static constexpr int TAB_OFF = B_OFF + NS * B_BYTES;
  static constexpr int NA = BM / 8, NBI = BN / 8;
  static constexpr int LA = ATR ? 0 : NA / 4;
  static constexpr int LB = (NBI + 3) / 4;
  static constexpr int NFULL = NBI % 4 == 0 ? 4 : NBI % 4;
  static constexpr int L = LA + LB;
  static constexpr int LDS = TAB_OFF + (ATR ? 8192 : 0);
  static_assert(NW == 8, "8 waves");
  static_assert(NA % 4 == 0, "A instr split");
  static_assert(NS >= 4, "stages");
  static_assert(LDS <= 163840, "lds");
};

template <class C, int BN, int WM, int WN, bool BKN>
struct GemmCtx {
  static constexpr int MT = C::MT, NT = C::NT, L = C::L, LA = C::LA, LB = C::LB;
  const char* Abase;
  const char* Bbase;
  size_t bstep;
  char* smem;
  unsigned goff[L > 0 ? L : 1];
  unsigned aoff[2], boff[2];
  int wq, par, last_n;
  bool full;

  __device__ __forceinline__ void init(const u16* Ab, int lda, const u16* Bb, int ldb, char* sm) {
    const int tid = threadIdx.x, lane = tid & 63;
    const int wave = __builtin_amdgcn_readfirstlane(tid >> 6);
    const int wm = wave % WM, wn = wave / WM;
    wq = wave & 3;
    par = wave >> 2;
    full = (C::NFULL == 4) || (wq < C::NFULL);
    smem = sm;
#pragma unroll
    for (int i = 0; i < LA; ++i) {
      int r = i * 4 + wq;
      int row = r * 8 + (lane >> 3);
      int lc = (lane & 7) ^ (row & 7);
      goff[i] = (unsigned)(row * lda + lc * 8) * 2u;
    }
#pragma unroll
    for (int i = 0; i < LB; ++i) {
      int r = i * 4 + wq;
      if (r >= C::NBI) r = 0;
      if (BKN) {
        int st = r * 4 + (lane >> 4);
        int kb = st / (BN / 16), nb = st % (BN / 16);
        int i16 = lane & 15, prow = i16 >> 1, hf = i16 & 1;
        int lrow = prow ^ ((kb & 1) << 2);
        goff[LA + i] = (unsigned)((kb * 8 + lrow) * ldb + nb * 16 + hf * 8) * 2u;
      } else {
        int row = r * 8 + (lane >> 3);
        int lc = (lane & 7) ^ (row & 7);
        goff[LA + i] = (unsigned)(row * ldb + lc * 8) * 2u;
      }
    }
    Abase = (const char*)Ab;
    Bbase = (const char*)Bb;
    bstep = BKN ? (size_t)64 * ldb * 2 : (size_t)128;
    const int g = lane >> 4, i16 = lane & 15;
#pragma unroll
    for (int s = 0; s < 2; ++s)
      aoff[s] = (unsigned)((wm * C::WTM + i16) * 128 + (((s * 4 + g) ^ (lane & 7)) << 4));
    if (BKN) {
      const int q4 = i16 >> 2, p = i16 & 3;
#pragma unroll
      for (int h = 0; h < 2; ++h)
        boff[h] = (unsigned)(C::B_OFF + (g * (BN / 16) + wn * NT) * 256 + (((h * 4 + q4) ^ ((g & 1) << 2)) * 32) +
                             p * 8);
    } else {
#pragma unroll
      for (int s = 0; s < 2; ++s)
        boff[s] = (unsigned)(C::B_OFF + (wn * C::WTN + i16) * 128 + (((s * 4 + g) ^ (lane & 7)) << 4));
    }
    const bool last_dummy = (C::NTT % WN != 0) && (wn * NT + NT > C::NTT);
    last_n = last_dummy ? 0 : NT - 1;
  }
  template <int i>
  __device__ __forceinline__ void issue1(int t, int sl) {
    if constexpr (i < LA) {
      __builtin_amdgcn_global_load_lds((const GAS unsigned*)(Abase + (size_t)t * 128 + goff[i]),
                                       (LAS unsigned*)(smem + sl * C::A_BYTES + wq * 1024 + i * 4096), 16, 0, 0);
    } else {
      if (i + 1 < L || full)
        __builtin_amdgcn_global_load_lds(
            (const GAS unsigned*)(Bbase + (size_t)t * bstep + goff[i]),
            (LAS unsigned*)(smem + C::B_OFF + sl * C::B_BYTES + wq * 1024 + (i - LA) * 4096), 16, 0, 0);
    }
  }
  __device__ __forceinline__ void issue_all(int t, int sl) {
    static_for<0, L>([&](auto II) { issue1<II.value>(t, sl); });
  }
  template <int n, int x>
  __device__ __forceinline__ void wait_st() {
    if (C::NFULL == 4 || n == 0) {
      waitv<n * L + x>();
    } else {
      if (full) waitv<n * L + x>();
      else waitv<n * (L - 1) + x>();
    }
  }
  template <int s>
  __device__ __forceinline__ void rd_frag(int sla, int slb, bf16x8 (&fa)[MT], bf16x8 (&fb)[NT]) {
    const char* sa = smem + sla * C::A_BYTES;
    const char* sb = smem + slb * C::B_BYTES;
#pragma unroll
    for (int m = 0; m < MT; ++m) fa[m] = *(const bf16x8*)(sa + aoff[s] + m * 2048);
#pragma unroll
    for (int n = 0; n < NT; ++n) {
      const int nn = (n == NT - 1) ? last_n : n;
      if (BKN) {
        bf16x4 lo = __builtin_amdgcn_ds_read_tr16_b64_v4bf16(
            (LAS bf16x4*)(sb + boff[0] + s * 4 * (BN / 16) * 256 + nn * 256));
        bf16x4 hi = __builtin_amdgcn_ds_read_tr16_b64_v4bf16(
            (LAS bf16x4*)(sb + boff[1] + s * 4 * (BN / 16) * 256 + nn * 256));
        fb[n] = __builtin_shufflevector(lo, hi, 0, 1, 2, 3, 4, 5, 6, 7);
      } else {
        fb[n] = *(const bf16x8*)(sb + boff[s] + nn * 2048);
      }
    }
  }
  template <bool ISS>
  __device__ __forceinline__ void mma(f32x4 (&acc)[MT][NT], bf16x8 (&fa)[MT], bf16x8 (&fb)[NT], int ti, int sl,
                                      bool do_issue) {
    static_for<0, MT * NT>([&](auto Q) {
      constexpr int q = Q.value, m = q / NT, n = q % NT;
      acc[m][n] = __builtin_amdgcn_mfma_f32_16x16x32_bf16(fb[n], fa[m], acc[m][n], 0, 0, 0);
      if constexpr (ISS) {
        constexpr int lo = (q * L) / (MT * NT), hi = ((q + 1) * L) / (MT * NT);
        if constexpr (hi > lo) {
          if (do_issue) static_for<lo, hi>([&](auto II) { issue1<II.value>(ti, sl); });
          __builtin_amdgcn_sched_barrier(0);
        }
      }
    });
  }
};

template <int BM, int BN, int WM, int WN, int NS, bool BKN, int T, int KID, class Pre, class Hook>
__device__ __forceinline__ void gemm_main(const u16* __restrict__ Ab, int lda, const u16* __restrict__ Bb, int ldb,
                                          char* smem,
                                          f32x4 (&acc)[GC<BM, BN, WM, WN, NS, BKN, false>::MT][GC<BM, BN, WM, WN, NS, BKN, false>::NT],
                                          Pre pre, Hook hook) {
  using C = GC<BM, BN, WM, WN, NS, BKN, false>;
  constexpr int MT = C::MT, NT = C::NT;
  GemmCtx<C, BN, WM, WN, BKN> g;
  g.init(Ab, lda, Bb, ldb, smem);
  STAMP(KID, 0);
  static_for<0, NS - 1>([&](auto S) {
    if ((S.value & 1) == g.par) g.issue_all(S.value, S.value);
  });
  pre();
  if (g.par == 0) g.template wait_st<(NS - 2) / 2, 0>();
  BARRIER();
  STAMP(KID, 1);
  bf16x8 fa0[MT], fb0[NT], fa1[MT], fb1[NT];
  g.template rd_frag<0>(0, 0, fa0, fb0);
  int sl = 0;
#pragma unroll 1
  for (int t = 0; t <= T - NS; ++t) {
    g.template rd_frag<1>(sl, sl, fa1, fb1);
    __builtin_amdgcn_sched_barrier(0);
    g.template mma<false>(acc, fa0, fb0, 0, 0, false);
    __builtin_amdgcn_sched_barrier(0);
    if (g.par == ((t + 1) & 1)) g.template wait_st<(NS - 3) / 2, 0>();
    BARRIER();
    const int sprev = sl == 0 ? NS - 1 : sl - 1;
    sl = sl == NS - 1 ? 0 : sl + 1;
    g.template rd_frag<0>(sl, sl, fa0, fb0);
    __builtin_amdgcn_sched_barrier(0);
    g.template mma<true>(acc, fa1, fb1, t + NS - 1, sprev, g.par == ((t + NS - 1) & 1));
    hook(t);
    __builtin_amdgcn_sched_barrier(0);
  }
  static_for<T - NS + 1, T>([&](auto TT) {
    constexpr int t = TT.value;
    g.template rd_frag<1>(t % NS, t % NS, fa1, fb1);
    __builtin_amdgcn_sched_barrier(0);
    g.template mma<false>(acc, fa0, fb0, 0, 0, false);
    __builtin_amdgcn_sched_barrier(0);
    if constexpr (t + 1 < T) {
      constexpr int younger = (T - 1) >= t + 3 ? (T - 1 - (t + 1)) / 2 : 0;
      if (g.par == ((t + 1) & 1)) g.template wait_st<younger, 0>();
      BARRIER();
      g.template rd_frag<0>((t + 1) % NS, (t + 1) % NS, fa0, fb0);
      __builtin_amdgcn_sched_barrier(0);
    }
    g.template mma<false>(acc, fa1, fb1, 0, 0, false);
    hook(t);
    __builtin_amdgcn_sched_barrier(0);
  });
  STAMP(KID, 2);
}

template <int IMM>
__device__ __forceinline__ void gld16(u32x4& dst, const void* sbase, unsigned voff) {
  asm volatile("global_load_dwordx4 %0, %1, %2 offset:%3" : "=v"(dst) : "v"(voff), "s"(sbase), "i"(IMM) : "memory");
}

template <int BN, int WM, int WN, int KID, class Stats>
__device__ __forceinline__ void gemm_atr(const u16* __restrict__ X, const u16* __restrict__ Bb, int ldb,
                                         const float* __restrict__ tab, char* smem,
                                         f32x4 (&acc)[GC<128, BN, WM, WN, 4, true, true>::MT][GC<128, BN, WM, WN, 4, true, true>::NT],
                                         Stats stats) {
  using C = GC<128, BN, WM, WN, 4, true, true>;
  constexpr int MT = C::MT, NT = C::NT, NS = 4, T = 12;
  GemmCtx<C, BN, WM, WN, true> g;
  g.init(X, DM, Bb, ldb, smem);
  const int tid = threadIdx.x;
  const int wave = __builtin_amdgcn_readfirstlane(tid >> 6);
  const int ch = tid & 7, r0 = tid >> 3;
  const unsigned xoff0 = (unsigned)(r0 * DM + ch * 8) * 2u, xoff1 = xoff0 + 64u * DM * 2u;
  const unsigned woff0 = (unsigned)(r0 * 128 + ((ch ^ (r0 & 7)) << 4)), woff1 = woff0 + 64 * 128;
  float* ltab = (float*)(smem + C::TAB_OFF);
  f32x2* lrow = (f32x2*)(smem + C::TAB_OFF + 6144);
  u32x4 xa[2][2];
  STAMP(KID, 0);
  if (wave < 6)
    __builtin_amdgcn_global_load_lds((const GAS unsigned*)((const char*)tab + wave * 1024 + (tid & 63) * 16),
                                     (LAS unsigned*)((char*)ltab + wave * 1024), 16, 0, 0);
  gld16<0>(xa[0][0], X, xoff0);
  gld16<0>(xa[0][1], X, xoff1);
  gld16<128>(xa[1][0], X, xoff0);
  gld16<128>(xa[1][1], X, xoff1);
  static_for<0, NS - 1>([&](auto S) {
    if ((S.value & 1) == g.par) g.issue_all(S.value, S.value);
  });
  stats(lrow);
  asm volatile("s_waitcnt vmcnt(0) lgkmcnt(0)" : "+v"(xa[0][0]), "+v"(xa[0][1]), "+v"(xa[1][0]), "+v"(xa[1][1])::"memory");
  BARRIER();
  const f32x2 rp0 = lrow[r0], rp1 = lrow[r0 + 64];

  auto transform = [&](auto TT, u32x4 (&xr)[2]) {
    constexpr int t = TT.value;
    const f32x4* ps = (const f32x4*)(ltab + t * 64 + ch * 8);
    const f32x4* pt = (const f32x4*)(ltab + DM + t * 64 + ch * 8);
    f32x4 s4[2] = {ps[0], ps[1]}, t4[2] = {pt[0], pt[1]};
    char* dst = smem + (t % 3) * C::A_BYTES;
#pragma unroll
    for (int r = 0; r < 2; ++r) {
      const f32x2 rp = r ? rp1 : rp0;
      u32x4 o;
#pragma unroll
      for (int e = 0; e < 4; ++e) {
        f32x2 x2 = {bflo(xr[r][e]), bfhi(xr[r][e])};
        f32x2 ss = {s4[e >> 1][(e & 1) * 2], s4[e >> 1][(e & 1) * 2 + 1]};
        f32x2 tt = {t4[e >> 1][(e & 1) * 2], t4[e >> 1][(e & 1) * 2 + 1]};
        f32x2 n2 = x2 * rp[0] + rp[1];
        f32x2 h2 = n2 * ss + tt;
        o[e] = pack2(h2[0], h2[1]);
      }
      *(u32x4*)(dst + (r ? woff1 : woff0)) = o;
    }
  };
  transform(IC<0>{}, xa[0]);
  transform(IC<1>{}, xa[1]);
  gld16<2 * 128>(xa[0][0], X, xoff0);
  gld16<2 * 128>(xa[0][1], X, xoff1);
  asm volatile("s_waitcnt lgkmcnt(0)" ::: "memory");
  BARRIER();
  STAMP(KID, 1);
  bf16x8 fa0[MT], fb0[NT], fa1[MT], fb1[NT];
  g.template rd_frag<0>(0, 0, fa0, fb0);
  static_for<0, T>([&](auto TT) {
    constexpr int t = TT.value;
    g.template rd_frag<1>(t % 3, t % NS, fa1, fb1);
    __builtin_amdgcn_sched_barrier(0);
    g.template mma<false>(acc, fa0, fb0, 0, 0, false);
    __builtin_amdgcn_sched_barrier(0);
    if constexpr (t + 1 < T) {
      if constexpr (t >= 1) {
        constexpr int na = (t + 2 < T) ? 2 : 0;
        constexpr bool b1 = (t >= 2);
        constexpr bool b2 = (t + 2 < T);
        if (g.par == ((t + 1) & 1)) {
          if constexpr (b1) g.template wait_st<1, na>();
          else g.template wait_st<0, na>();
        } else {
          if constexpr (b2) g.template wait_st<1, na>();
          else g.template wait_st<0, na>();
        }
        asm volatile("" : "+v"(xa[(t + 1) & 1][0]), "+v"(xa[(t + 1) & 1][1]));
        __builtin_amdgcn_sched_barrier(0);
        transform(IC<t + 1>{}, xa[(t + 1) & 1]);
      }
      if (g.par == ((t + 1) & 1)) {
        constexpr int na = (t + 2 < T && t >= 1) ? 2 : ((t == 0) ? 2 : 0);
        if constexpr (t == 0) {
        } else {
          g.template wait_st<0, na>();
        }
      }
      asm volatile("s_waitcnt lgkmcnt(0)" ::: "memory");
      BARRIER();
      g.template rd_frag<0>((t + 1) % 3, (t + 1) % NS, fa0, fb0);
      if constexpr (t + 3 < T) {
        gld16<(t + 3) * 128>(xa[(t + 3) & 1][0], X, xoff0);
        gld16<(t + 3) * 128>(xa[(t + 3) & 1][1], X, xoff1);
      }
      __builtin_amdgcn_sched_barrier(0);
    }
    if constexpr (t + 3 < T) g.template mma<true>(acc, fa1, fb1, t + 3, (t + 3) % NS, g.par == ((t + 3) & 1));
    else g.template mma<false>(acc, fa1, fb1, 0, 0, false);
    __builtin_amdgcn_sched_barrier(0);
  });
  STAMP(KID, 2);
}

struct NoHook {
  __device__ __forceinline__ void operator()(int) const {}
  __device__ __forceinline__ void operator()() const {}
};

__device__ __forceinline__ f32x2 ln_params(float mean, float var) {
  const float mb = rbf(mean), vb = rbf(var);
  const float sd = rbf(__fsqrt_rn(rbf(vb + 1e-5f)));
  const float ra = 1.f / sd;
  return f32x2{ra, -mb * ra};
}
__global__ __launch_bounds__(256) void k_pre(const u16* __restrict__ c, const u16* __restrict__ wmod,
                                             const u16* __restrict__ x, float* __restrict__ gates,
                                             float* __restrict__ tab, f32x2* __restrict__ st1) {
  __shared__ float red[2][64][33];
  const int tid = threadIdx.x;
  if (blockIdx.x < 144) {
    const int cg = tid & 3, kq = tid >> 2;
    const int col0 = blockIdx.x * 32 + cg * 8;
    u32x4 w[12];
#pragma unroll
    for (int i = 0; i < 12; ++i) {
      int k = i * 64 + kq;
      w[i] = *(const u32x4*)(wmod + (size_t)k * (6 * DM) + col0);
    }
    float a0[8], a1[8];
#pragma unroll
    for (int j = 0; j < 8; ++j) a0[j] = a1[j] = 0.f;
#pragma unroll
    for (int i = 0; i < 12; ++i) {
      int k = i * 64 + kq;
      float c0 = bf2f(c[k]), c1 = bf2f(c[DM + k]);
      float s0 = rbf(c0 / (1.f + __expf(-c0)));
      float s1 = rbf(c1 / (1.f + __expf(-c1)));
#pragma unroll
      for (int j = 0; j < 4; ++j) {
        float wl = bflo(w[i][j]), wh = bfhi(w[i][j]);
        a0[2 * j] += s0 * wl; a0[2 * j + 1] += s0 * wh;
        a1[2 * j] += s1 * wl; a1[2 * j + 1] += s1 * wh;
      }
    }
#pragma unroll
    for (int j = 0; j < 8; ++j) {
      red[0][kq][cg * 8 + j] = a0[j];
      red[1][kq][cg * 8 + j] = a1[j];
    }
    __syncthreads();
    if (tid < 64) {
      int b = tid >> 5, cl = tid & 31;
      float s = 0.f;
      for (int q = 0; q < 64; ++q) s += red[b][q][cl];
      const float v = rbf(s);
      const int col = blockIdx.x * 32 + cl, i = col / DM, cc = col % DM;
      const int w_ = i / 3, kind = i % 3;
      if (kind == 2) gates[(b * 2 + w_) * DM + cc] = v;
      else if (kind == 1) tab[((b * 2 + w_) * 2 + 0) * DM + cc] = rbf(1.f + v);
      else tab[((b * 2 + w_) * 2 + 1) * DM + cc] = v;
    }
  } else {
    const int lane = tid & 63, wave = tid >> 6;
    const int rbase = (blockIdx.x - 144) * 16 + wave * 4;
    u32x2 u[4][3];
#pragma unroll
    for (int r = 0; r < 4; ++r)
#pragma unroll
      for (int i = 0; i < 3; ++i) u[r][i] = *(const u32x2*)(x + (size_t)(rbase + r) * DM + i * 256 + lane * 4);
#pragma unroll
    for (int r = 0; r < 4; ++r) {
      float v[12];
#pragma unroll
      for (int i = 0; i < 3; ++i) {
        v[i * 4 + 0] = bflo(u[r][i][0]); v[i * 4 + 1] = bfhi(u[r][i][0]);
        v[i * 4 + 2] = bflo(u[r][i][1]); v[i * 4 + 3] = bfhi(u[r][i][1]);
      }
      float s = 0.f;
#pragma unroll
      for (int i = 0; i < 12; ++i) s += v[i];
#pragma unroll
      for (int o = 32; o; o >>= 1) s += __shfl_xor(s, o);
      const float mean = s * (1.f / DM);
      float q = 0.f;
#pragma unroll
      for (int i = 0; i < 12; ++i) { float d = v[i] - mean; q += d * d; }
#pragma unroll
      for (int o = 32; o; o >>= 1) q += __shfl_xor(q, o);
      if (lane == 0) st1[rbase + r] = ln_params(mean, q * (1.f / DM));
    }
  }
}

using GQKV = GC<128, 144, 4, 2, 4, true, true>;
__global__ __launch_bounds__(512, 2) void k_qkv(const u16* __restrict__ x, const u16* __restrict__ w,
                                                const float* __restrict__ tab, const f32x2* __restrict__ st1,
                                                u16* __restrict__ qkv) {
  extern __shared__ __attribute__((aligned(16))) char smem[];
  using C = GQKV;
  constexpr int BM = 128, BN = 144;
  const int xc = blockIdx.x & 7, j = blockIdx.x >> 3;
  const int tm = (xc >> 2) * 8 + (j >> 2), tn = (xc & 3) * 4 + (j & 3);
  const int m0 = tm * BM, n0 = tn * BN, b = m0 >> 10;
  f32x4 acc[C::MT][C::NT] = {};
  auto stats = [&](f32x2* lrow) {
    if (threadIdx.x < 128) lrow[threadIdx.x] = st1[m0 + threadIdx.x];
  };
  gemm_atr<BN, 4, 2, 0>(x + (size_t)m0 * DM, w + n0, 3 * DM, tab + (size_t)(b * 2 + 0) * 2 * DM, smem, acc, stats);
  const int lane = threadIdx.x & 63, wave = threadIdx.x >> 6, wm = wave % 4, wn = wave / 4;
#pragma unroll
  for (int m = 0; m < C::MT; ++m)
#pragma unroll
    for (int n = 0; n < C::NT; ++n) {
      if (wn * C::NT + n < C::NTT) {
        int row = m0 + wm * C::WTM + m * 16 + (lane & 15);
        int col = n0 + wn * C::WTN + n * 16 + (lane >> 4) * 4;
        u32x2 v = {pack2(acc[m][n][0], acc[m][n][1]), pack2(acc[m][n][2], acc[m][n][3])};
        *(u32x2*)(qkv + (size_t)row * (3 * DM) + col) = v;
      }
    }
  STAMP(0, 3);
}

using GSC = GC<128, 128, 4, 2, 4, false, false>;
__global__ __launch_bounds__(512, 2) void k_scores(const u16* __restrict__ qkv, u16* __restrict__ E,
                                                   f32x2* __restrict__ st) {
  extern __shared__ __attribute__((aligned(16))) char smem[];
  using C = GSC;
  constexpr int BM = 128, BN = 128;
  const int xc = blockIdx.x & 7, j = blockIdx.x >> 3;
  const int bh = xc >> 1, b = bh >> 1, hh = bh & 1;
  const int tm = (xc & 1) * 4 + (j >> 3), tn = j & 7;
  const int m0 = tm * BM, n0 = tn * BN;
  const u16* Q = qkv + (size_t)(b * NSEQ + m0) * (3 * DM) + hh * DH;
  const u16* K = qkv + (size_t)(b * NSEQ + n0) * (3 * DM) + DM + hh * DH;
  f32x4 acc[C::MT][C::NT] = {};
  gemm_main<BM, BN, 4, 2, 4, false, 6, 1>(Q, 3 * DM, K, 3 * DM, smem, acc, NoHook(), NoHook());
  const int lane = threadIdx.x & 63, wave = threadIdx.x >> 6, wm = wave % 4, wn = wave / 4;
  const float scale = 0.05103103630798288f;
#pragma unroll
  for (int m = 0; m < C::MT; ++m) {
    float s[C::NT][4];
    float mx = -INFINITY;
#pragma unroll
    for (int n = 0; n < C::NT; ++n) {
      unsigned p0 = pack2(acc[m][n][0], acc[m][n][1]), p1 = pack2(acc[m][n][2], acc[m][n][3]);
      p0 = pack2(bflo(p0) * scale, bfhi(p0) * scale);
      p1 = pack2(bflo(p1) * scale, bfhi(p1) * scale);
      s[n][0] = bflo(p0); s[n][1] = bfhi(p0); s[n][2] = bflo(p1); s[n][3] = bfhi(p1);
      mx = fmaxf(mx, fmaxf(fmaxf(s[n][0], s[n][1]), fmaxf(s[n][2], s[n][3])));
    }
    mx = fmaxf(mx, __shfl_xor(mx, 16));
    mx = fmaxf(mx, __shfl_xor(mx, 32));
    float sum = 0.f;
    const int row = m0 + wm * C::WTM + m * 16 + (lane & 15);
    u16* Er = E + ((size_t)bh * NSEQ + row) * NSEQ + n0 + wn * C::WTN + (lane >> 4) * 4;
#pragma unroll
    for (int n = 0; n < C::NT; ++n) {
#pragma unroll
      for (int e = 0; e < 4; ++e) {
        s[n][e] = __expf(s[n][e] - mx);
        sum += s[n][e];
      }
      u32x2 v = {pack2(s[n][0], s[n][1]), pack2(s[n][2], s[n][3])};
      *(u32x2*)(Er + n * 16) = v;
    }
    sum += __shfl_xor(sum, 16);
    sum += __shfl_xor(sum, 32);
    if (lane < 16) st[((size_t)bh * NSEQ + row) * 16 + tn * 2 + wn] = f32x2{mx, sum};
  }
  STAMP(1, 3);
}

using GPV = GC<64, 96, 4, 2, 6, true, false>;
__global__ __launch_bounds__(512, 2) void k_pv(const u16* __restrict__ E, const f32x2* __restrict__ st,
                                               const u16* __restrict__ qkv, u16* __restrict__ O) {
  extern __shared__ __attribute__((aligned(16))) char smem[];
  using C = GPV;
  constexpr int BM = 64, BN = 96;
  const int xc = blockIdx.x & 7, j = blockIdx.x >> 3;
  const int bh = xc >> 1, b = bh >> 1, hh = bh & 1;
  const int tm = (xc & 1) * 8 + (j >> 2), tn = j & 3;
  const int m0 = tm * BM, n0 = tn * BN;
  const int lane = threadIdx.x & 63, wave = threadIdx.x >> 6, wm = wave % 4, wn = wave / 4;
  float* ftab = (float*)(smem + C::LDS);
  auto pre = [&]() {
    if (threadIdx.x < 64) {
      const f32x4* p = (const f32x4*)(st + ((size_t)bh * NSEQ + m0 + threadIdx.x) * 16);
      f32x4 v[8];
#pragma unroll
      for (int t = 0; t < 8; ++t) v[t] = p[t];
      float M = -INFINITY;
#pragma unroll
      for (int t = 0; t < 8; ++t) M = fmaxf(M, fmaxf(v[t][0], v[t][2]));
      float fac[16], Ls = 0.f;
#pragma unroll
      for (int t = 0; t < 8; ++t) {
        fac[2 * t] = __expf(v[t][0] - M);
        fac[2 * t + 1] = __expf(v[t][2] - M);
        Ls += fac[2 * t] * v[t][1] + fac[2 * t + 1] * v[t][3];
      }
      float inv = 1.f / Ls;
#pragma unroll
      for (int t = 0; t < 16; ++t) ftab[t * 64 + threadIdx.x] = fac[t] * inv;
      asm volatile("s_waitcnt lgkmcnt(0)" ::: "memory");
    }
  };
  const u16* A = E + ((size_t)bh * NSEQ + m0) * NSEQ;
  const u16* V = qkv + (size_t)(b * NSEQ) * (3 * DM) + 2 * DM + hh * DH + n0;
  f32x4 acc[1][3] = {};
  f32x4 tot[1][3] = {};
  const float* frow = ftab + wm * 16 + (lane & 15);
  auto hook = [&](int t) {
    const float f = frow[t * 64];
#pragma unroll
    for (int n = 0; n < 3; ++n) {
      tot[0][n] += acc[0][n] * f;
      acc[0][n] = f32x4{0.f, 0.f, 0.f, 0.f};
    }
  };
  gemm_main<BM, BN, 4, 2, 6, true, 16, 2>(A, NSEQ, V, 3 * DM, smem, acc, pre, hook);
#pragma unroll
  for (int n = 0; n < 3; ++n) {
    int row = b * NSEQ + m0 + wm * 16 + (lane & 15);
    int col = hh * DH + n0 + wn * C::WTN + n * 16 + (lane >> 4) * 4;
    u32x2 v = {pack2(tot[0][n][0], tot[0][n][1]), pack2(tot[0][n][2], tot[0][n][3])};
    *(u32x2*)(O + (size_t)row * DM + col) = v;
  }
  STAMP(2, 3);
}

template <int KI, int LDA, bool STATS>
__device__ __forceinline__ void res_gemm(const u16* __restrict__ A, const u16* __restrict__ W,
                                         const u16* __restrict__ xin, const float* __restrict__ gates,
                                         u16* __restrict__ xo, f32x2* __restrict__ pst, char* smem) {
  using C = GPV;
  constexpr int BM = 64, BN = 96;
  const int xc = blockIdx.x & 7, j = blockIdx.x >> 3;
  const int tm = (xc >> 1) * 8 + (j >> 2), tn = (xc & 1) * 4 + (j & 3);
  const int m0 = tm * BM, n0 = tn * BN, b = m0 >> 10;
  const int lane = threadIdx.x & 63, wave = threadIdx.x >> 6, wm = wave % 4, wn = wave / 4;
  const int row = m0 + wm * 16 + (lane & 15);
  const int col0 = n0 + wn * C::WTN + (lane >> 4) * 4;
  f32x4 acc[1][3] = {};
  gemm_main<BM, BN, 4, 2, 6, true, LDA / 64, KI>(A + (size_t)m0 * LDA, LDA, W + n0, DM, smem, acc, NoHook(), NoHook());
  float vals[12];
#pragma unroll
  for (int n = 0; n < 3; ++n) {
    u32x2 xv = *(const u32x2*)(xin + (size_t)row * DM + col0 + n * 16);
    f32x4 gt = *(const f32x4*)(gates + b * (2 * DM) + col0 + n * 16);
    unsigned a0 = pack2(acc[0][n][0], acc[0][n][1]), a1 = pack2(acc[0][n][2], acc[0][n][3]);
    unsigned g0 = pack2(gt[0] * bflo(a0), gt[1] * bfhi(a0));
    unsigned g1 = pack2(gt[2] * bflo(a1), gt[3] * bfhi(a1));
    u32x2 o = {pack2(bflo(xv[0]) + bflo(g0), bfhi(xv[0]) + bfhi(g0)),
               pack2(bflo(xv[1]) + bflo(g1), bfhi(xv[1]) + bfhi(g1))};
    *(u32x2*)(xo + (size_t)row * DM + col0 + n * 16) = o;
    vals[n * 4 + 0] = bflo(o[0]); vals[n * 4 + 1] = bfhi(o[0]);
    vals[n * 4 + 2] = bflo(o[1]); vals[n * 4 + 3] = bfhi(o[1]);
  }
  if (STATS) {
    float s = 0.f;
#pragma unroll
    for (int i = 0; i < 12; ++i) s += vals[i];
    s += __shfl_xor(s, 16);
    s += __shfl_xor(s, 32);
    const float lm = s * (1.f / 48.f);
    float q = 0.f;
#pragma unroll
    for (int i = 0; i < 12; ++i) { float d = vals[i] - lm; q += d * d; }
    q += __shfl_xor(q, 16);
    q += __shfl_xor(q, 32);
    if (lane < 16) pst[(size_t)row * 16 + tn * 2 + wn] = f32x2{s, q};
  }
  STAMP(KI, 3);
}
__global__ __launch_bounds__(512, 2) void k_wo(const u16* __restrict__ O, const u16* __restrict__ wo,
                                               const u16* __restrict__ xin, const float* __restrict__ gates,
                                               u16* __restrict__ x1, f32x2* __restrict__ pst) {
  extern __shared__ __attribute__((aligned(16))) char smem[];
  res_gemm<3, DM, true>(O, wo, xin, gates, x1, pst, smem);
}
__global__ __launch_bounds__(512, 2) void k_fc2(const u16* __restrict__ f, const u16* __restrict__ w2,
                                                const u16* __restrict__ x1, const float* __restrict__ gates,
                                                u16* __restrict__ out) {
  extern __shared__ __attribute__((aligned(16))) char smem[];
  res_gemm<5, FF, false>(f, w2, x1, gates + DM, out, nullptr, smem);
}

__device__ __forceinline__ float gelu_tanh(float u) {
  float w = u * (-2.f * 0.7978845608028654f * 1.4426950408889634f);
  float a = w * (1.f + 0.044715f * u * u);
  return u * __builtin_amdgcn_rcpf(1.f + __builtin_amdgcn_exp2f(a));
}
using GFC = GC<128, 192, 2, 4, 4, true, true>;
__global__ __launch_bounds__(512, 2) void k_fc1(const u16* __restrict__ x1, const u16* __restrict__ w1,
                                                const float* __restrict__ tab, const f32x2* __restrict__ pst,
                                                u16* __restrict__ f) {
  extern __shared__ __attribute__((aligned(16))) char smem[];
  using C = GFC;
  constexpr int BM = 128, BN = 192;
  const int xc = blockIdx.x & 7, j = blockIdx.x >> 3;
  const int tm = (xc >> 2) * 8 + (j >> 2), tn = (xc & 3) * 4 + (j & 3);
  const int m0 = tm * BM, n0 = tn * BN, b = m0 >> 10;
  f32x4 acc[C::MT][C::NT] = {};
  auto stats = [&](f32x2* lrow) {
    if (threadIdx.x < 128) {
      const f32x4* p = (const f32x4*)(pst + (size_t)(m0 + threadIdx.x) * 16);
      f32x4 v[8];
#pragma unroll
      for (int i = 0; i < 8; ++i) v[i] = p[i];
      float s = 0.f;
#pragma unroll
      for (int i = 0; i < 8; ++i) s += v[i][0] + v[i][2];
      const float mean = s * (1.f / DM);
      float q = 0.f;
#pragma unroll
      for (int i = 0; i < 8; ++i) {
        float d0 = v[i][0] * (1.f / 48.f) - mean, d1 = v[i][2] * (1.f / 48.f) - mean;
        q += v[i][1] + v[i][3] + 48.f * (d0 * d0 + d1 * d1);
      }
      lrow[threadIdx.x] = ln_params(mean, q * (1.f / DM));
    }
  };
  gemm_atr<BN, 2, 4, 4>(x1 + (size_t)m0 * DM, w1 + n0, FF, tab + (size_t)(b * 2 + 1) * 2 * DM, smem, acc, stats);
  const int lane = threadIdx.x & 63, wave = threadIdx.x >> 6, wm = wave % 2, wn = wave / 2;
#pragma unroll
  for (int m = 0; m < C::MT; ++m)
#pragma unroll
    for (int n = 0; n < C::NT; ++n) {
      int row = m0 + wm * C::WTM + m * 16 + (lane & 15);
      int col = n0 + wn * C::WTN + n * 16 + (lane >> 4) * 4;
      unsigned p0 = pack2(acc[m][n][0], acc[m][n][1]), p1 = pack2(acc[m][n][2], acc[m][n][3]);
      u32x2 v = {pack2(gelu_tanh(bflo(p0)), gelu_tanh(bfhi(p0))), pack2(gelu_tanh(bflo(p1)), gelu_tanh(bfhi(p1)))};
      *(u32x2*)(f + (size_t)row * FF + col) = v;
    }
  STAMP(4, 3);
}

extern "C" void kernel_launch(void* const* d_in, const int* in_sizes, int n_in, void* d_out, int out_size,
                              void* d_ws, size_t ws_size, hipStream_t stream) {
  const u16* x = (const u16*)d_in[0];
  const u16* c = (const u16*)d_in[1];
  const u16* wqkv = (const u16*)d_in[2];
  const u16* wo = (const u16*)d_in[3];
  const u16* wmod = (const u16*)d_in[4];
  const u16* w1 = (const u16*)d_in[5];
  const u16* w2 = (const u16*)d_in[6];
  u16* out = (u16*)d_out;

  char* ws = (char*)d_ws;
  size_t o = 0;
  auto take = [&](size_t bytes) { char* p = ws + o; o += (bytes + 255) & ~(size_t)255; return p; };
  float* gates = (float*)take(2 * 2 * DM * 4);
  float* tab = (float*)take(2 * 2 * 2 * DM * 4);
  f32x2* st1 = (f32x2*)take((size_t)MROWS * 8);
  u16* qkv = (u16*)take((size_t)MROWS * 3 * DM * 2);
  u16* E = (u16*)take((size_t)4 * NSEQ * NSEQ * 2);
  f32x2* st = (f32x2*)take((size_t)4 * NSEQ * 16 * 8);
  u16* O = (u16*)take((size_t)MROWS * DM * 2);
  u16* x1 = (u16*)take((size_t)MROWS * DM * 2);
  f32x2* pst = (f32x2*)take((size_t)MROWS * 16 * 8);
  u16* f = (u16*)take((size_t)MROWS * FF * 2);

  constexpr int LDS_QKV = GQKV::LDS;
  constexpr int LDS_SC = GSC::LDS;
  constexpr int LDS_PV = GPV::LDS + 16 * 64 * 4;
  constexpr int LDS_WO = GPV::LDS;
  constexpr int LDS_FC = GFC::LDS;
  (void)hipFuncSetAttribute((const void*)k_qkv, hipFuncAttributeMaxDynamicSharedMemorySize, LDS_QKV);
  (void)hipFuncSetAttribute((const void*)k_scores, hipFuncAttributeMaxDynamicSharedMemorySize, LDS_SC);
  (void)hipFuncSetAttribute((const void*)k_pv, hipFuncAttributeMaxDynamicSharedMemorySize, LDS_PV);
  (void)hipFuncSetAttribute((const void*)k_wo, hipFuncAttributeMaxDynamicSharedMemorySize, LDS_WO);
  (void)hipFuncSetAttribute((const void*)k_fc1, hipFuncAttributeMaxDynamicSharedMemorySize, LDS_FC);
  (void)hipFuncSetAttribute((const void*)k_fc2, hipFuncAttributeMaxDynamicSharedMemorySize, LDS_WO);

  k_pre<<<144 + 128, 256, 0, stream>>>(c, wmod, x, gates, tab, st1);
  k_qkv<<<256, 512, LDS_QKV, stream>>>(x, wqkv, tab, st1, qkv);
  k_scores<<<256, 512, LDS_SC, stream>>>(qkv, E, st);
  k_pv<<<256, 512, LDS_PV, stream>>>(E, st, qkv, O);
  k_wo<<<256, 512, LDS_WO, stream>>>(O, wo, x, gates, x1, pst);
  k_fc1<<<256, 512, LDS_FC, stream>>>(x1, w1, tab, pst, f);
  k_fc2<<<256, 512, LDS_WO, stream>>>(f, w2, x1, gates, out);
}
```

```cpp
#include <hip/hip_runtime.h>
#include <hip/hip_bf16.h>
#include <stdint.h>

typedef unsigned short u16;
typedef __attribute__((ext_vector_type(8))) __bf16 bf16x8;
typedef __attribute__((ext_vector_type(4))) __bf16 bf16x4;
typedef __attribute__((ext_vector_type(2))) __bf16 bf16x2;
typedef __attribute__((ext_vector_type(4))) float f32x4;
typedef __attribute__((ext_vector_type(2))) float f32x2;
typedef __attribute__((ext_vector_type(4))) unsigned u32x4;
typedef __attribute__((ext_vector_type(2))) unsigned u32x2;
#define LAS __attribute__((address_space(3)))
#define GAS __attribute__((address_space(1)))

constexpr int NB = 2, NSEQ = 1024, DM = 768, NH = 2, DH = 384, FF = 3072;
constexpr int MROWS = NB * NSEQ;

__device__ __forceinline__ float bf2f(u16 v) { return __uint_as_float(((unsigned)v) << 16); }
__device__ __forceinline__ float bflo(unsigned v) { return __uint_as_float(v << 16); }
__device__ __forceinline__ float bfhi(unsigned v) { return __uint_as_float(v & 0xffff0000u); }
__device__ __forceinline__ unsigned pack2(float a, float b) {
  f32x2 f = {a, b};
  bf16x2 r = __builtin_convertvector(f, bf16x2);
  return __builtin_bit_cast(unsigned, r);
}
__device__ __forceinline__ float rbf(float a) {
  return bflo(pack2(a, 0.f) & 0xffffu);
}

template <int N>
__device__ __forceinline__ void waitv() {
  asm volatile("s_waitcnt vmcnt(%0)" ::"i"(N) : "memory");
}
template <int I>
struct IC {
  static constexpr int value = I;
};
template <int B, int E, class F>
__device__ __forceinline__ void static_for(F&& f) {
  if constexpr (B < E) {
    f(IC<B>{});
    static_for<B + 1, E>(f);
  }
}
#define ABL 0
#define FENCE() asm volatile("" ::: "memory")
#define STAMP(kid, i)
#define BARRIER()                        \
  do {                                   \
    FENCE();                             \
    __builtin_amdgcn_s_barrier();        \
    FENCE();                             \
  } while (0)

template <int BM, int BN, int WM, int WN, int NS, bool BKN, bool ATR>
struct GC {
  static constexpr int NW = WM * WN;
  static constexpr int WTM = BM / WM, MT = WTM / 16;
  static constexpr int NTT = BN / 16;
  static constexpr int NT = (NTT + WN - 1) / WN;
  static constexpr int WTN = NT * 16;
  static constexpr int A_BYTES = BM * 128, B_BYTES = BN * 128;
  static constexpr int NSA = ATR ? 3 : NS;
  static constexpr int B_OFF = NSA * A_BYTES;
  static constexpr int TAB_OFF = B_OFF + NS * B_BYTES;
  static constexpr int NA = BM / 8, NBI = BN / 8;
  static constexpr int LA = ATR ? 0 : NA / 4;
  static constexpr int LB = (NBI + 3) / 4;
  static constexpr int NFULL = NBI % 4 == 0 ? 4 : NBI % 4;
  static constexpr int L = LA + LB;
  static constexpr int LDS = TAB_OFF + (ATR ? 8192 : 0);
  static_assert(NW == 8, "8 waves");
  static_assert(NA % 4 == 0, "A instr split");
  static_assert(NS >= 4, "stages");
  static_assert(LDS <= 163840, "lds");
};

template <class C, int BN, int WM, int WN, bool BKN>
struct GemmCtx {
  static constexpr int MT = C::MT, NT = C::NT, L = C::L, LA = C::LA, LB = C::LB;
  const char* Abase;
  const char* Bbase;
  size_t bstep;
  char* smem;
  unsigned goff[L > 0 ? L : 1];
  unsigned aoff[2], boff[2];
  int wq, par, last_n;
  bool full;

  __device__ __forceinline__ void init(const u16* Ab, int lda, const u16* Bb, int ldb, char* sm) {
    const int tid = threadIdx.x, lane = tid & 63;
    const int wave = __builtin_amdgcn_readfirstlane(tid >> 6);
    const int wm = wave % WM, wn = wave / WM;
    wq = wave & 3;
    par = wave >> 2;
    full = (C::NFULL == 4) || (wq < C::NFULL);
    smem = sm;
#pragma unroll
    for (int i = 0; i < LA; ++i) {
      int r = i * 4 + wq;
      int row = r * 8 + (lane >> 3);
      int lc = (lane & 7) ^ (row & 7);
      goff[i] = (unsigned)(row * lda + lc * 8) * 2u;
    }
#pragma unroll
    for (int i = 0; i < LB; ++i) {
      int r = i * 4 + wq;
      if (r >= C::NBI) r = 0;
      if (BKN) {
        int st = r * 4 + (lane >> 4);
        int kb = st / (BN / 16), nb = st % (BN / 16);
        int i16 = lane & 15, prow = i16 >> 1, hf = i16 & 1;
        int lrow = prow ^ ((kb & 1) << 2);
        goff[LA + i] = (unsigned)((kb * 8 + lrow) * ldb + nb * 16 + hf * 8) * 2u;
      } else {
        int row = r * 8 + (lane >> 3);
        int lc = (lane & 7) ^ (row & 7);
        goff[LA + i] = (unsigned)(row * ldb + lc * 8) * 2u;
      }
    }
    Abase = (const char*)Ab;
    Bbase = (const char*)Bb;
    bstep = BKN ? (size_t)64 * ldb * 2 : (size_t)128;
    const int g = lane >> 4, i16 = lane & 15;
#pragma unroll
    for (int s = 0; s < 2; ++s)
      aoff[s] = (unsigned)((wm * C::WTM + i16) * 128 + (((s * 4 + g) ^ (lane & 7)) << 4));
    if (BKN) {
      const int q4 = i16 >> 2, p = i16 & 3;
#pragma unroll
      for (int h = 0; h < 2; ++h)
        boff[h] = (unsigned)(C::B_OFF + (g * (BN / 16) + wn * NT) * 256 + (((h * 4 + q4) ^ ((g & 1) << 2)) * 32) +
                             p * 8);
    } else {
#pragma unroll
      for (int s = 0; s < 2; ++s)
        boff[s] = (unsigned)(C::B_OFF + (wn * C::WTN + i16) * 128 + (((s * 4 + g) ^ (lane & 7)) << 4));
    }
    const bool last_dummy = (C::NTT % WN != 0) && (wn * NT + NT > C::NTT);
    last_n = last_dummy ? 0 : NT - 1;
  }
  template <int i>
  __device__ __forceinline__ void issue1(int t, int sl) {
    if constexpr (i < LA) {
      __builtin_amdgcn_global_load_lds((const GAS unsigned*)(Abase + (size_t)t * 128 + goff[i]),
                                       (LAS unsigned*)(smem + sl * C::A_BYTES + wq * 1024 + i * 4096), 16, 0, 0);
    } else {
      if (i + 1 < L || full)
        __builtin_amdgcn_global_load_lds(
            (const GAS unsigned*)(Bbase + (size_t)t * bstep + goff[i]),
            (LAS unsigned*)(smem + C::B_OFF + sl * C::B_BYTES + wq * 1024 + (i - LA) * 4096), 16, 0, 0);
    }
  }
  __device__ __forceinline__ void issue_all(int t, int sl) {
    static_for<0, L>([&](auto II) { issue1<II.value>(t, sl); });
  }
  template <int n, int x>
  __device__ __forceinline__ void wait_st() {
    if (C::NFULL == 4 || n == 0) {
      waitv<n * L + x>();
    } else {
      if (full) waitv<n * L + x>();
      else waitv<n * (L - 1) + x>();
    }
  }
  template <int s>
  __device__ __forceinline__ void rd_frag(int sla, int slb, bf16x8 (&fa)[MT], bf16x8 (&fb)[NT]) {
    const char* sa = smem + sla * C::A_BYTES;
    const char* sb = smem + slb * C::B_BYTES;
#pragma unroll
    for (int m = 0; m < MT; ++m) fa[m] = *(const bf16x8*)(sa + aoff[s] + m * 2048);
#pragma unroll
    for (int n = 0; n < NT; ++n) {
      const int nn = (n == NT - 1) ? last_n : n;
      if (BKN) {
        bf16x4 lo = __builtin_amdgcn_ds_read_tr16_b64_v4bf16(
            (LAS bf16x4*)(sb + boff[0] + s * 4 * (BN / 16) * 256 + nn * 256));
        bf16x4 hi = __builtin_amdgcn_ds_read_tr16_b64_v4bf16(
            (LAS bf16x4*)(sb + boff[1] + s * 4 * (BN / 16) * 256 + nn * 256));
        fb[n] = __builtin_shufflevector(lo, hi, 0, 1, 2, 3, 4, 5, 6, 7);
      } else {
        fb[n] = *(const bf16x8*)(sb + boff[s] + nn * 2048);
      }
    }
  }
  template <bool ISS>
  __device__ __forceinline__ void mma(f32x4 (&acc)[MT][NT], bf16x8 (&fa)[MT], bf16x8 (&fb)[NT], int ti, int sl,
                                      bool do_issue) {
    static_for<0, MT * NT>([&](auto Q) {
      constexpr int q = Q.value, m = q / NT, n = q % NT;
      acc[m][n] = __builtin_amdgcn_mfma_f32_16x16x32_bf16(fb[n], fa[m], acc[m][n], 0, 0, 0);
      if constexpr (ISS) {
        constexpr int lo = (q * L) / (MT * NT), hi = ((q + 1) * L) / (MT * NT);
        if constexpr (hi > lo) {
          if (do_issue) static_for<lo, hi>([&](auto II) { issue1<II.value>(ti, sl); });
          __builtin_amdgcn_sched_barrier(0);
        }
      }
    });
  }
};

template <int BM, int BN, int WM, int WN, int NS, bool BKN, int T, int KID, class Pre, class Hook>
__device__ __forceinline__ void gemm_main(const u16* __restrict__ Ab, int lda, const u16* __restrict__ Bb, int ldb,
                                          char* smem,
                                          f32x4 (&acc)[GC<BM, BN, WM, WN, NS, BKN, false>::MT][GC<BM, BN, WM, WN, NS, BKN, false>::NT],
                                          Pre pre, Hook hook) {
  using C = GC<BM, BN, WM, WN, NS, BKN, false>;
  constexpr int MT = C::MT, NT = C::NT;
  GemmCtx<C, BN, WM, WN, BKN> g;
  g.init(Ab, lda, Bb, ldb, smem);
  STAMP(KID, 0);
  static_for<0, NS - 1>([&](auto S) {
    if ((S.value & 1) == g.par) g.issue_all(S.value, S.value);
  });
  pre();
  if (g.par == 0) g.template wait_st<(NS - 2) / 2, 0>();
  BARRIER();
  STAMP(KID, 1);
  bf16x8 fa0[MT], fb0[NT], fa1[MT], fb1[NT];
  g.template rd_frag<0>(0, 0, fa0, fb0);
  constexpr int U = (NS % 2 == 0) ? NS : 2 * NS;
  constexpr int NSTEADY = T - NS + 1;
  constexpr int NBLK = NSTEADY / U;
#pragma unroll 1
  for (int tb = 0; tb < NBLK * U; tb += U) {
    static_for<0, U>([&](auto UU) {
      constexpr int u = UU.value;
      const int t = tb + u;
      g.template rd_frag<1>(u % NS, u % NS, fa1, fb1);
      __builtin_amdgcn_sched_barrier(0);
      g.template mma<false>(acc, fa0, fb0, 0, 0, false);
      __builtin_amdgcn_sched_barrier(0);
      if (g.par == ((u + 1) & 1)) g.template wait_st<(NS - 3) / 2, 0>();
      BARRIER();
      g.template rd_frag<0>((u + 1) % NS, (u + 1) % NS, fa0, fb0);
      __builtin_amdgcn_sched_barrier(0);
      g.template mma<true>(acc, fa1, fb1, t + NS - 1, (u + NS - 1) % NS, g.par == ((u + NS - 1) & 1));
      hook(t);
      __builtin_amdgcn_sched_barrier(0);
    });
  }
  static_for<NBLK * U, NSTEADY>([&](auto TT) {
    constexpr int t = TT.value;
    g.template rd_frag<1>(t % NS, t % NS, fa1, fb1);
    __builtin_amdgcn_sched_barrier(0);
    g.template mma<false>(acc, fa0, fb0, 0, 0, false);
    __builtin_amdgcn_sched_barrier(0);
    if (g.par == ((t + 1) & 1)) g.template wait_st<(NS - 3) / 2, 0>();
    BARRIER();
    g.template rd_frag<0>((t + 1) % NS, (t + 1) % NS, fa0, fb0);
    __builtin_amdgcn_sched_barrier(0);
    g.template mma<true>(acc, fa1, fb1, t + NS - 1, (t + NS - 1) % NS, g.par == ((t + NS - 1) & 1));
    hook(t);
    __builtin_amdgcn_sched_barrier(0);
  });
  static_for<T - NS + 1, T>([&](auto TT) {
    constexpr int t = TT.value;
    g.template rd_frag<1>(t % NS, t % NS, fa1, fb1);
    __builtin_amdgcn_sched_barrier(0);
    g.template mma<false>(acc, fa0, fb0, 0, 0, false);
    __builtin_amdgcn_sched_barrier(0);
    if constexpr (t + 1 < T) {
      constexpr int younger = (T - 1) >= t + 3 ? (T - 1 - (t + 1)) / 2 : 0;
      if (g.par == ((t + 1) & 1)) g.template wait_st<younger, 0>();
      BARRIER();
      g.template rd_frag<0>((t + 1) % NS, (t + 1) % NS, fa0, fb0);
      __builtin_amdgcn_sched_barrier(0);
    }
    g.template mma<false>(acc, fa1, fb1, 0, 0, false);
    hook(t);
    __builtin_amdgcn_sched_barrier(0);
  });
  STAMP(KID, 2);
}

struct NoHook {
  __device__ __forceinline__ void operator()(int) const {}
  __device__ __forceinline__ void operator()() const {}
};

__device__ __forceinline__ f32x2 ln_params(float mean, float var) {
  const float mb = rbf(mean), vb = rbf(var);
  const float sd = rbf(__fsqrt_rn(rbf(vb + 1e-5f)));
  const float ra = 1.f / sd;
  return f32x2{ra, -mb * ra};
}
__global__ __launch_bounds__(256) void k_pre(const u16* __restrict__ c, const u16* __restrict__ wmod,
                                             float* __restrict__ gates, float* __restrict__ tab,
                                             const u16* __restrict__ pw0, const u16* __restrict__ pw1,
                                             const u16* __restrict__ pw2, const u16* __restrict__ pw3) {
  __shared__ float red[2][64][33];
  const int tid = threadIdx.x;
  if (blockIdx.x < 144) {
    const int cg = tid & 3, kq = tid >> 2;
    const int col0 = blockIdx.x * 32 + cg * 8;
    u32x4 w[12];
#pragma unroll
    for (int i = 0; i < 12; ++i) {
      int k = i * 64 + kq;
      w[i] = *(const u32x4*)(wmod + (size_t)k * (6 * DM) + col0);
    }
    float a0[8], a1[8];
#pragma unroll
    for (int j = 0; j < 8; ++j) a0[j] = a1[j] = 0.f;
#pragma unroll
    for (int i = 0; i < 12; ++i) {
      int k = i * 64 + kq;
      float c0 = bf2f(c[k]), c1 = bf2f(c[DM + k]);
      float s0 = rbf(c0 / (1.f + __expf(-c0)));
      float s1 = rbf(c1 / (1.f + __expf(-c1)));
#pragma unroll
      for (int j = 0; j < 4; ++j) {
        float wl = bflo(w[i][j]), wh = bfhi(w[i][j]);
        a0[2 * j] += s0 * wl; a0[2 * j + 1] += s0 * wh;
        a1[2 * j] += s1 * wl; a1[2 * j + 1] += s1 * wh;
      }
    }
#pragma unroll
    for (int j = 0; j < 8; ++j) {
      red[0][kq][cg * 8 + j] = a0[j];
      red[1][kq][cg * 8 + j] = a1[j];
    }
    __syncthreads();
    if (tid < 64) {
      int b = tid >> 5, cl = tid & 31;
      float s = 0.f;
      for (int q = 0; q < 64; ++q) s += red[b][q][cl];
      const float v = rbf(s);
      const int col = blockIdx.x * 32 + cl, i = col / DM, cc = col % DM;
      const int w_ = i / 3, kind = i % 3;
      if (kind == 2) gates[(b * 2 + w_) * DM + cc] = v;
      else if (kind == 1) tab[((b * 2 + w_) * 2 + 0) * DM + cc] = rbf(1.f + v);
      else tab[((b * 2 + w_) * 2 + 1) * DM + cc] = v;
    }
  } else {
    const int wb = blockIdx.x - 144;
    const u16* wp[4] = {pw0, pw1, pw2, pw3};
    const int nlines[4] = {DM * 3 * DM / 64, DM * DM / 64, DM * FF / 64, FF * DM / 64};
    unsigned accu = 0;
#pragma unroll
    for (int q = 0; q < 4; ++q) {
      for (int l = wb * 256 + tid; l < nlines[q]; l += 112 * 256) accu += *(const unsigned*)(wp[q] + (size_t)l * 64);
    }
    if (accu == 0x9e3779b9u && tid == 999) gates[0] = 0.f;
  }
}

__global__ __launch_bounds__(256) void k_lnmod(const u16* __restrict__ x, const float* __restrict__ tab, int which,
                                               u16* __restrict__ out) {
  const int lane = threadIdx.x & 63, row = blockIdx.x * 4 + (threadIdx.x >> 6);
  const int b = row >> 10;
  const u16* xr = x + (size_t)row * DM;
  float v[12];
#pragma unroll
  for (int i = 0; i < 3; ++i) {
    u32x2 u = *(const u32x2*)(xr + i * 256 + lane * 4);
    v[i * 4 + 0] = bflo(u[0]); v[i * 4 + 1] = bfhi(u[0]);
    v[i * 4 + 2] = bflo(u[1]); v[i * 4 + 3] = bfhi(u[1]);
  }
  const float* S = tab + (size_t)(b * 2 + which) * 2 * DM;
  const float* Tt = S + DM;
  f32x4 sc[3], sh[3];
#pragma unroll
  for (int i = 0; i < 3; ++i) {
    sc[i] = *(const f32x4*)(S + i * 256 + lane * 4);
    sh[i] = *(const f32x4*)(Tt + i * 256 + lane * 4);
  }
  float s = 0.f;
#pragma unroll
  for (int i = 0; i < 12; ++i) s += v[i];
#pragma unroll
  for (int o = 32; o; o >>= 1) s += __shfl_xor(s, o);
  const float mean = s * (1.f / DM);
  float q = 0.f;
#pragma unroll
  for (int i = 0; i < 12; ++i) { float d = v[i] - mean; q += d * d; }
#pragma unroll
  for (int o = 32; o; o >>= 1) q += __shfl_xor(q, o);
  const float mb = rbf(mean), vb = rbf(q * (1.f / DM));
  const float sd = rbf(__fsqrt_rn(rbf(vb + 1e-5f)));
#pragma unroll
  for (int i = 0; i < 3; ++i) {
    float h[4];
#pragma unroll
    for (int j = 0; j < 4; ++j) {
      float n = rbf(rbf(v[i * 4 + j] - mb) / sd);
      h[j] = rbf(n * sc[i][j]) + sh[i][j];
    }
    u32x2 o = {pack2(h[0], h[1]), pack2(h[2], h[3])};
    *(u32x2*)(out + (size_t)row * DM + i * 256 + lane * 4) = o;
  }
}

using GQKV = GC<128, 144, 4, 2, 4, true, false>;
__global__ __launch_bounds__(512, 2) void k_qkv(const u16* __restrict__ h, const u16* __restrict__ w,
                                                u16* __restrict__ qkv) {
  extern __shared__ __attribute__((aligned(16))) char smem[];
  using C = GQKV;
  constexpr int BM = 128, BN = 144;
  const int xc = blockIdx.x & 7, j = blockIdx.x >> 3;
  const int tm = (xc >> 2) * 8 + (j >> 2), tn = (xc & 3) * 4 + (j & 3);
  const int m0 = tm * BM, n0 = tn * BN;
  f32x4 acc[C::MT][C::NT] = {};
  gemm_main<BM, BN, 4, 2, 4, true, 12, 0>(h + (size_t)m0 * DM, DM, w + n0, 3 * DM, smem, acc, NoHook(), NoHook());
  const int lane = threadIdx.x & 63, wave = threadIdx.x >> 6, wm = wave % 4, wn = wave / 4;
#pragma unroll
  for (int m = 0; m < C::MT; ++m)
#pragma unroll
    for (int n = 0; n < C::NT; ++n) {
      if (wn * C::NT + n < C::NTT) {
        int row = m0 + wm * C::WTM + m * 16 + (lane & 15);
        int col = n0 + wn * C::WTN + n * 16 + (lane >> 4) * 4;
        u32x2 v = {pack2(acc[m][n][0], acc[m][n][1]), pack2(acc[m][n][2], acc[m][n][3])};
        *(u32x2*)(qkv + (size_t)row * (3 * DM) + col) = v;
      }
    }
  STAMP(0, 3);
}

using GSC = GC<128, 128, 4, 2, 4, false, false>;
__global__ __launch_bounds__(512, 2) void k_scores(const u16* __restrict__ qkv, u16* __restrict__ E,
                                                   f32x2* __restrict__ st) {
  extern __shared__ __attribute__((aligned(16))) char smem[];
  using C = GSC;
  constexpr int BM = 128, BN = 128;
  const int xc = blockIdx.x & 7, j = blockIdx.x >> 3;
  const int bh = xc >> 1, b = bh >> 1, hh = bh & 1;
  const int tm = (xc & 1) * 4 + (j >> 3), tn = j & 7;
  const int m0 = tm * BM, n0 = tn * BN;
  const u16* Q = qkv + (size_t)(b * NSEQ + m0) * (3 * DM) + hh * DH;
  const u16* K = qkv + (size_t)(b * NSEQ + n0) * (3 * DM) + DM + hh * DH;
  f32x4 acc[C::MT][C::NT] = {};
  gemm_main<BM, BN, 4, 2, 4, false, 6, 1>(Q, 3 * DM, K, 3 * DM, smem, acc, NoHook(), NoHook());
  const int lane = threadIdx.x & 63, wave = threadIdx.x >> 6, wm = wave % 4, wn = wave / 4;
  const float scale = 0.05103103630798288f;
#pragma unroll
  for (int m = 0; m < C::MT; ++m) {
    float s[C::NT][4];
    float mx = -INFINITY;
#pragma unroll
    for (int n = 0; n < C::NT; ++n) {
      unsigned p0 = pack2(acc[m][n][0], acc[m][n][1]), p1 = pack2(acc[m][n][2], acc[m][n][3]);
      p0 = pack2(bflo(p0) * scale, bfhi(p0) * scale);
      p1 = pack2(bflo(p1) * scale, bfhi(p1) * scale);
      s[n][0] = bflo(p0); s[n][1] = bfhi(p0); s[n][2] = bflo(p1); s[n][3] = bfhi(p1);
      mx = fmaxf(mx, fmaxf(fmaxf(s[n][0], s[n][1]), fmaxf(s[n][2], s[n][3])));
    }
    mx = fmaxf(mx, __shfl_xor(mx, 16));
    mx = fmaxf(mx, __shfl_xor(mx, 32));
    float sum = 0.f;
    const int row = m0 + wm * C::WTM + m * 16 + (lane & 15);
    u16* Er = E + ((size_t)bh * NSEQ + row) * NSEQ + n0 + wn * C::WTN + (lane >> 4) * 4;
#pragma unroll
    for (int n = 0; n < C::NT; ++n) {
#pragma unroll
      for (int e = 0; e < 4; ++e) {
        s[n][e] = __expf(s[n][e] - mx);
        sum += s[n][e];
      }
      u32x2 v = {pack2(s[n][0], s[n][1]), pack2(s[n][2], s[n][3])};
      *(u32x2*)(Er + n * 16) = v;
    }
    sum += __shfl_xor(sum, 16);
    sum += __shfl_xor(sum, 32);
    if (lane < 16) st[((size_t)bh * NSEQ + row) * 16 + tn * 2 + wn] = f32x2{mx, sum};
  }
  STAMP(1, 3);
}

using GPV = GC<64, 96, 4, 2, 6, true, false>;
__global__ __launch_bounds__(512, 2) void k_pv(const u16* __restrict__ E, const f32x2* __restrict__ st,
                                               const u16* __restrict__ qkv, u16* __restrict__ O) {
  extern __shared__ __attribute__((aligned(16))) char smem[];
  using C = GPV;
  constexpr int BM = 64, BN = 96;
  const int xc = blockIdx.x & 7, j = blockIdx.x >> 3;
  const int bh = xc >> 1, b = bh >> 1, hh = bh & 1;
  const int tm = (xc & 1) * 8 + (j >> 2), tn = j & 3;
  const int m0 = tm * BM, n0 = tn * BN;
  const int lane = threadIdx.x & 63, wave = threadIdx.x >> 6, wm = wave % 4, wn = wave / 4;
  float* ftab = (float*)(smem + C::LDS);
  auto pre = [&]() {
    if (threadIdx.x < 64) {
      const f32x4* p = (const f32x4*)(st + ((size_t)bh * NSEQ + m0 + threadIdx.x) * 16);
      f32x4 v[8];
#pragma unroll
      for (int t = 0; t < 8; ++t) v[t] = p[t];
      float M = -INFINITY;
#pragma unroll
      for (int t = 0; t < 8; ++t) M = fmaxf(M, fmaxf(v[t][0], v[t][2]));
      float fac[16], Ls = 0.f;
#pragma unroll
      for (int t = 0; t < 8; ++t) {
        fac[2 * t] = __expf(v[t][0] - M);
        fac[2 * t + 1] = __expf(v[t][2] - M);
        Ls += fac[2 * t] * v[t][1] + fac[2 * t + 1] * v[t][3];
      }
      float inv = 1.f / Ls;
#pragma unroll
      for (int t = 0; t < 16; ++t) ftab[t * 64 + threadIdx.x] = fac[t] * inv;
      asm volatile("s_waitcnt lgkmcnt(0)" ::: "memory");
    }
  };
  const u16* A = E + ((size_t)bh * NSEQ + m0) * NSEQ;
  const u16* V = qkv + (size_t)(b * NSEQ) * (3 * DM) + 2 * DM + hh * DH + n0;
  f32x4 acc[1][3] = {};
  f32x4 tot[1][3] = {};
  const float* frow = ftab + wm * 16 + (lane & 15);
  auto hook = [&](int t) {
    const float f = frow[t * 64];
#pragma unroll
    for (int n = 0; n < 3; ++n) {
      tot[0][n] += acc[0][n] * f;
      acc[0][n] = f32x4{0.f, 0.f, 0.f, 0.f};
    }
  };
  gemm_main<BM, BN, 4, 2, 6, true, 16, 2>(A, NSEQ, V, 3 * DM, smem, acc, pre, hook);
#pragma unroll
  for (int n = 0; n < 3; ++n) {
    int row = b * NSEQ + m0 + wm * 16 + (lane & 15);
    int col = hh * DH + n0 + wn * C::WTN + n * 16 + (lane >> 4) * 4;
    u32x2 v = {pack2(tot[0][n][0], tot[0][n][1]), pack2(tot[0][n][2], tot[0][n][3])};
    *(u32x2*)(O + (size_t)row * DM + col) = v;
  }
  STAMP(2, 3);
}

template <int KI, int LDA, bool STATS>
__device__ __forceinline__ void res_gemm(const u16* __restrict__ A, const u16* __restrict__ W,
                                         const u16* __restrict__ xin, const float* __restrict__ gates,
                                         u16* __restrict__ xo, f32x2* __restrict__ pst, char* smem) {
  using C = GPV;
  constexpr int BM = 64, BN = 96;
  const int xc = blockIdx.x & 7, j = blockIdx.x >> 3;
  const int tm = (xc >> 1) * 8 + (j >> 2), tn = (xc & 1) * 4 + (j & 3);
  const int m0 = tm * BM, n0 = tn * BN, b = m0 >> 10;
  const int lane = threadIdx.x & 63, wave = threadIdx.x >> 6, wm = wave % 4, wn = wave / 4;
  const int row = m0 + wm * 16 + (lane & 15);
  const int col0 = n0 + wn * C::WTN + (lane >> 4) * 4;
  f32x4 acc[1][3] = {};
  gemm_main<BM, BN, 4, 2, 6, true, LDA / 64, KI>(A + (size_t)m0 * LDA, LDA, W + n0, DM, smem, acc, NoHook(), NoHook());
  float vals[12];
#pragma unroll
  for (int n = 0; n < 3; ++n) {
    u32x2 xv = *(const u32x2*)(xin + (size_t)row * DM + col0 + n * 16);
    f32x4 gt = *(const f32x4*)(gates + b * (2 * DM) + col0 + n * 16);
    unsigned a0 = pack2(acc[0][n][0], acc[0][n][1]), a1 = pack2(acc[0][n][2], acc[0][n][3]);
    unsigned g0 = pack2(gt[0] * bflo(a0), gt[1] * bfhi(a0));
    unsigned g1 = pack2(gt[2] * bflo(a1), gt[3] * bfhi(a1));
    u32x2 o = {pack2(bflo(xv[0]) + bflo(g0), bfhi(xv[0]) + bfhi(g0)),
               pack2(bflo(xv[1]) + bflo(g1), bfhi(xv[1]) + bfhi(g1))};
    *(u32x2*)(xo + (size_t)row * DM + col0 + n * 16) = o;
    vals[n * 4 + 0] = bflo(o[0]); vals[n * 4 + 1] = bfhi(o[0]);
    vals[n * 4 + 2] = bflo(o[1]); vals[n * 4 + 3] = bfhi(o[1]);
  }
  if (STATS) {
    float s = 0.f;
#pragma unroll
    for (int i = 0; i < 12; ++i) s += vals[i];
    s += __shfl_xor(s, 16);
    s += __shfl_xor(s, 32);
    const float lm = s * (1.f / 48.f);
    float q = 0.f;
#pragma unroll
    for (int i = 0; i < 12; ++i) { float d = vals[i] - lm; q += d * d; }
    q += __shfl_xor(q, 16);
    q += __shfl_xor(q, 32);
    if (lane < 16) pst[(size_t)row * 16 + tn * 2 + wn] = f32x2{s, q};
  }
  STAMP(KI, 3);
}
__global__ __launch_bounds__(512, 2) void k_wo(const u16* __restrict__ O, const u16* __restrict__ wo,
                                               const u16* __restrict__ xin, const float* __restrict__ gates,
                                               u16* __restrict__ x1, f32x2* __restrict__ pst) {
  extern __shared__ __attribute__((aligned(16))) char smem[];
  res_gemm<3, DM, false>(O, wo, xin, gates, x1, pst, smem);
}
__global__ __launch_bounds__(512, 2) void k_fc2(const u16* __restrict__ f, const u16* __restrict__ w2,
                                                const u16* __restrict__ x1, const float* __restrict__ gates,
                                                u16* __restrict__ out) {
  extern __shared__ __attribute__((aligned(16))) char smem[];
  res_gemm<5, FF, false>(f, w2, x1, gates + DM, out, nullptr, smem);
}

__device__ __forceinline__ float gelu_tanh(float u) {
  float w = u * (-2.f * 0.7978845608028654f * 1.4426950408889634f);
  float a = w * (1.f + 0.044715f * u * u);
  return u * __builtin_amdgcn_rcpf(1.f + __builtin_amdgcn_exp2f(a));
}
using GFC = GC<128, 192, 2, 4, 4, true, false>;
__global__ __launch_bounds__(512, 2) void k_fc1(const u16* __restrict__ h2, const u16* __restrict__ w1,
                                                u16* __restrict__ f) {
  extern __shared__ __attribute__((aligned(16))) char smem[];
  using C = GFC;
  constexpr int BM = 128, BN = 192;
  const int xc = blockIdx.x & 7, j = blockIdx.x >> 3;
  const int tm = (xc >> 2) * 8 + (j >> 2), tn = (xc & 3) * 4 + (j & 3);
  const int m0 = tm * BM, n0 = tn * BN;
  f32x4 acc[C::MT][C::NT] = {};
  gemm_main<BM, BN, 2, 4, 4, true, 12, 4>(h2 + (size_t)m0 * DM, DM, w1 + n0, FF, smem, acc, NoHook(), NoHook());
  const int lane = threadIdx.x & 63, wave = threadIdx.x >> 6, wm = wave % 2, wn = wave / 2;
#pragma unroll
  for (int m = 0; m < C::MT; ++m)
#pragma unroll
    for (int n = 0; n < C::NT; ++n) {
      int row = m0 + wm * C::WTM + m * 16 + (lane & 15);
      int col = n0 + wn * C::WTN + n * 16 + (lane >> 4) * 4;
      unsigned p0 = pack2(acc[m][n][0], acc[m][n][1]), p1 = pack2(acc[m][n][2], acc[m][n][3]);
      u32x2 v = {pack2(gelu_tanh(bflo(p0)), gelu_tanh(bfhi(p0))), pack2(gelu_tanh(bflo(p1)), gelu_tanh(bfhi(p1)))};
      *(u32x2*)(f + (size_t)row * FF + col) = v;
    }
  STAMP(4, 3);
}

extern "C" void kernel_launch(void* const* d_in, const int* in_sizes, int n_in, void* d_out, int out_size,
                              void* d_ws, size_t ws_size, hipStream_t stream) {
  const u16* x = (const u16*)d_in[0];
  const u16* c = (const u16*)d_in[1];
  const u16* wqkv = (const u16*)d_in[2];
  const u16* wo = (const u16*)d_in[3];
  const u16* wmod = (const u16*)d_in[4];
  const u16* w1 = (const u16*)d_in[5];
  const u16* w2 = (const u16*)d_in[6];
  u16* out = (u16*)d_out;

  char* ws = (char*)d_ws;
  size_t o = 0;
  auto take = [&](size_t bytes) { char* p = ws + o; o += (bytes + 255) & ~(size_t)255; return p; };
  float* gates = (float*)take(2 * 2 * DM * 4);
  float* tab = (float*)take(2 * 2 * 2 * DM * 4);
  u16* h = (u16*)take((size_t)MROWS * DM * 2);
  u16* h2 = (u16*)take((size_t)MROWS * DM * 2);
  u16* qkv = (u16*)take((size_t)MROWS * 3 * DM * 2);
  u16* E = (u16*)take((size_t)4 * NSEQ * NSEQ * 2);
  f32x2* st = (f32x2*)take((size_t)4 * NSEQ * 16 * 8);
  u16* O = (u16*)take((size_t)MROWS * DM * 2);
  u16* x1 = (u16*)take((size_t)MROWS * DM * 2);
  f32x2* pst = (f32x2*)take((size_t)MROWS * 16 * 8);
  u16* f = (u16*)take((size_t)MROWS * FF * 2);

  constexpr int LDS_QKV = GQKV::LDS;
  constexpr int LDS_SC = GSC::LDS;
  constexpr int LDS_PV = GPV::LDS + 16 * 64 * 4;
  constexpr int LDS_WO = GPV::LDS;
  constexpr int LDS_FC = GFC::LDS;
  (void)hipFuncSetAttribute((const void*)k_qkv, hipFuncAttributeMaxDynamicSharedMemorySize, LDS_QKV);
  (void)hipFuncSetAttribute((const void*)k_scores, hipFuncAttributeMaxDynamicSharedMemorySize, LDS_SC);
  (void)hipFuncSetAttribute((const void*)k_pv, hipFuncAttributeMaxDynamicSharedMemorySize, LDS_PV);
  (void)hipFuncSetAttribute((const void*)k_wo, hipFuncAttributeMaxDynamicSharedMemorySize, LDS_WO);
  (void)hipFuncSetAttribute((const void*)k_fc1, hipFuncAttributeMaxDynamicSharedMemorySize, LDS_FC);
  (void)hipFuncSetAttribute((const void*)k_fc2, hipFuncAttributeMaxDynamicSharedMemorySize, LDS_WO);

  k_pre<<<144 + 112, 256, 0, stream>>>(c, wmod, gates, tab, wqkv, wo, w1, w2);
  k_lnmod<<<MROWS / 4, 256, 0, stream>>>(x, tab, 0, h);
  k_qkv<<<256, 512, LDS_QKV, stream>>>(h, wqkv, qkv);
  k_scores<<<256, 512, LDS_SC, stream>>>(qkv, E, st);
  k_pv<<<256, 512, LDS_PV, stream>>>(E, st, qkv, O);
  k_wo<<<256, 512, LDS_WO, stream>>>(O, wo, x, gates, x1, pst);
  k_lnmod<<<MROWS / 4, 256, 0, stream>>>(x1, tab, 1, h2);
  k_fc1<<<256, 512, LDS_FC, stream>>>(h2, w1, f);
  k_fc2<<<256, 512, LDS_WO, stream>>>(f, w2, x1, gates, out);
}
```

```cpp
#include <hip/hip_runtime.h>
#include <hip/hip_bf16.h>
#include <stdint.h>

typedef unsigned short u16;
typedef __attribute__((ext_vector_type(8))) __bf16 bf16x8;
typedef __attribute__((ext_vector_type(4))) __bf16 bf16x4;
typedef __attribute__((ext_vector_type(2))) __bf16 bf16x2;
typedef __attribute__((ext_vector_type(4))) float f32x4;
typedef __attribute__((ext_vector_type(2))) float f32x2;
typedef __attribute__((ext_vector_type(4))) unsigned u32x4;
typedef __attribute__((ext_vector_type(2))) unsigned u32x2;
#define LAS __attribute__((address_space(3)))
#define GAS __attribute__((address_space(1)))

constexpr int NB = 2, NSEQ = 1024, DM = 768, NH = 2, DH = 384, FF = 3072;
constexpr int MROWS = NB * NSEQ;

__device__ __forceinline__ float bf2f(u16 v) { return __uint_as_float(((unsigned)v) << 16); }
__device__ __forceinline__ float bflo(unsigned v) { return __uint_as_float(v << 16); }
__device__ __forceinline__ float bfhi(unsigned v) { return __uint_as_float(v & 0xffff0000u); }
__device__ __forceinline__ unsigned pack2(float a, float b) {
  f32x2 f = {a, b};
  bf16x2 r = __builtin_convertvector(f, bf16x2);
  return __builtin_bit_cast(unsigned, r);
}
__device__ __forceinline__ float rbf(float a) {
  return bflo(pack2(a, 0.f) & 0xffffu);
}

template <int N>
__device__ __forceinline__ void waitv() {
  asm volatile("s_waitcnt vmcnt(%0)" ::"i"(N) : "memory");
}
template <int I>
struct IC {
  static constexpr int value = I;
};
template <int B, int E, class F>
__device__ __forceinline__ void static_for(F&& f) {
  if constexpr (B < E) {
    f(IC<B>{});
    static_for<B + 1, E>(f);
  }
}
#define ABL 0
#define FENCE() asm volatile("" ::: "memory")
#define STAMP(kid, i)
#define BARRIER()                        \
  do {                                   \
    FENCE();                             \
    __builtin_amdgcn_s_barrier();        \
    FENCE();                             \
  } while (0)

template <int BM, int BN, int WM, int WN, int NS, bool BKN, bool ATR>
struct GC {
  static constexpr int NW = WM * WN;
  static constexpr int WTM = BM / WM, MT = WTM / 16;
  static constexpr int NTT = BN / 16;
  static constexpr int NT = (NTT + WN - 1) / WN;
  static constexpr int WTN = NT * 16;
  static constexpr int A_BYTES = BM * 128, B_BYTES = BN * 128;
  static constexpr int NSA = ATR ? 3 : NS;
  static constexpr int B_OFF = NSA * A_BYTES;
  static constexpr int TAB_OFF = B_OFF + NS * B_BYTES;
  static constexpr int NA = BM / 8, NBI = BN / 8;
  static constexpr int LA = ATR ? 0 : NA / 4;
  static constexpr int LB = (NBI + 3) / 4;
  static constexpr int NFULL = NBI % 4 == 0 ? 4 : NBI % 4;
  static constexpr int L = LA + LB;
  static constexpr int LDS = TAB_OFF + (ATR ? 8192 : 0);
  static_assert(NW == 8, "8 waves");
  static_assert(NA % 4 == 0, "A instr split");
  static_assert(NS >= 4, "stages");
  static_assert(LDS <= 163840, "lds");
};

template <class C, int BN, int WM, int WN, bool BKN>
struct GemmCtx {
  static constexpr int MT = C::MT, NT = C::NT, L = C::L, LA = C::LA, LB = C::LB;
  const char* Abase;
  const char* Bbase;
  size_t bstep;
  char* smem;
  unsigned goff[L > 0 ? L : 1];
  unsigned aoff[2], boff[2];
  int wq, par, last_n;
  bool full;

  __device__ __forceinline__ void init(const u16* Ab, int lda, const u16* Bb, int ldb, char* sm) {
    const int tid = threadIdx.x, lane = tid & 63;
    const int wave = __builtin_amdgcn_readfirstlane(tid >> 6);
    const int wm = wave % WM, wn = wave / WM;
    wq = wave & 3;
    par = wave >> 2;
    full = (C::NFULL == 4) || (wq < C::NFULL);
    smem = sm;
#pragma unroll
    for (int i = 0; i < LA; ++i) {
      int r = i * 4 + wq;
      int row = r * 8 + (lane >> 3);
      int lc = (lane & 7) ^ (row & 7);
      goff[i] = (unsigned)(row * lda + lc * 8) * 2u;
    }
#pragma unroll
    for (int i = 0; i < LB; ++i) {
      int r = i * 4 + wq;
      if (r >= C::NBI) r = 0;
      if (BKN) {
        int st = r * 4 + (lane >> 4);
        int kb = st / (BN / 16), nb = st % (BN / 16);
        int i16 = lane & 15, prow = i16 >> 1, hf = i16 & 1;
        int lrow = prow ^ ((kb & 1) << 2);
        goff[LA + i] = (unsigned)((kb * 8 + lrow) * ldb + nb * 16 + hf * 8) * 2u;
      } else {
        int row = r * 8 + (lane >> 3);
        int lc = (lane & 7) ^ (row & 7);
        goff[LA + i] = (unsigned)(row * ldb + lc * 8) * 2u;
      }
    }
    Abase = (const char*)Ab;
    Bbase = (const char*)Bb;
    bstep = BKN ? (size_t)64 * ldb * 2 : (size_t)128;
    const int g = lane >> 4, i16 = lane & 15;
#pragma unroll
    for (int s = 0; s < 2; ++s)
      aoff[s] = (unsigned)((wm * C::WTM + i16) * 128 + (((s * 4 + g) ^ (lane & 7)) << 4));
    if (BKN) {
      const int q4 = i16 >> 2, p = i16 & 3;
#pragma unroll
      for (int h = 0; h < 2; ++h)
        boff[h] = (unsigned)(C::B_OFF + (g * (BN / 16) + wn * NT) * 256 + (((h * 4 + q4) ^ ((g & 1) << 2)) * 32) +
                             p * 8);
    } else {
#pragma unroll
      for (int s = 0; s < 2; ++s)
        boff[s] = (unsigned)(C::B_OFF + (wn * C::WTN + i16) * 128 + (((s * 4 + g) ^ (lane & 7)) << 4));
    }
    const bool last_dummy = (C::NTT % WN != 0) && (wn * NT + NT > C::NTT);
    last_n = last_dummy ? 0 : NT - 1;
  }
  template <int i>
  __device__ __forceinline__ void issue1(int t, int sl) {
    if constexpr (i < LA) {
      __builtin_amdgcn_global_load_lds((const GAS unsigned*)(Abase + (size_t)t * 128 + goff[i]),
                                       (LAS unsigned*)(smem + sl * C::A_BYTES + wq * 1024 + i * 4096), 16, 0, 0);
    } else {
      if (i + 1 < L || full)
        __builtin_amdgcn_global_load_lds(
            (const GAS unsigned*)(Bbase + (size_t)t * bstep + goff[i]),
            (LAS unsigned*)(smem + C::B_OFF + sl * C::B_BYTES + wq * 1024 + (i - LA) * 4096), 16, 0, 0);
    }
  }
  __device__ __forceinline__ void issue_all(int t, int sl) {
    static_for<0, L>([&](auto II) { issue1<II.value>(t, sl); });
  }
  template <int n, int x>
  __device__ __forceinline__ void wait_st() {
    if (C::NFULL == 4 || n == 0) {
      waitv<n * L + x>();
    } else {
      if (full) waitv<n * L + x>();
      else waitv<n * (L - 1) + x>();
    }
  }
  template <int s>
  __device__ __forceinline__ void rd_frag(int sla, int slb, bf16x8 (&fa)[MT], bf16x8 (&fb)[NT]) {
    const char* sa = smem + sla * C::A_BYTES;
    const char* sb = smem + slb * C::B_BYTES;
#pragma unroll
    for (int m = 0; m < MT; ++m) fa[m] = *(const bf16x8*)(sa + aoff[s] + m * 2048);
#pragma unroll
    for (int n = 0; n < NT; ++n) {
      const int nn = (n == NT - 1) ? last_n : n;
      if (BKN) {
        bf16x4 lo = __builtin_amdgcn_ds_read_tr16_b64_v4bf16(
            (LAS bf16x4*)(sb + boff[0] + s * 4 * (BN / 16) * 256 + nn * 256));
        bf16x4 hi = __builtin_amdgcn_ds_read_tr16_b64_v4bf16(
            (LAS bf16x4*)(sb + boff[1] + s * 4 * (BN / 16) * 256 + nn * 256));
        fb[n] = __builtin_shufflevector(lo, hi, 0, 1, 2, 3, 4, 5, 6, 7);
      } else {
        fb[n] = *(const bf16x8*)(sb + boff[s] + nn * 2048);
      }
    }
  }
  template <bool ISS>
  __device__ __forceinline__ void mma(f32x4 (&acc)[MT][NT], bf16x8 (&fa)[MT], bf16x8 (&fb)[NT], int ti, int sl,
                                      bool do_issue) {
    static_for<0, MT * NT>([&](auto Q) {
      constexpr int q = Q.value, m = q / NT, n = q % NT;
      acc[m][n] = __builtin_amdgcn_mfma_f32_16x16x32_bf16(fb[n], fa[m], acc[m][n], 0, 0, 0);
      if constexpr (ISS) {
        constexpr int lo = (q * L) / (MT * NT), hi = ((q + 1) * L) / (MT * NT);
        if constexpr (hi > lo) {
          if (do_issue) static_for<lo, hi>([&](auto II) { issue1<II.value>(ti, sl); });
          __builtin_amdgcn_sched_barrier(0);
        }
      }
    });
  }
};

template <int BM, int BN, int WM, int WN, int NS, bool BKN, int T, int KID, class Pre, class Hook>
__device__ __forceinline__ void gemm_main(const u16* __restrict__ Ab, int lda, const u16* __restrict__ Bb, int ldb,
                                          char* smem,
                                          f32x4 (&acc)[GC<BM, BN, WM, WN, NS, BKN, false>::MT][GC<BM, BN, WM, WN, NS, BKN, false>::NT],
                                          Pre pre, Hook hook) {
  using C = GC<BM, BN, WM, WN, NS, BKN, false>;
  constexpr int MT = C::MT, NT = C::NT;
  GemmCtx<C, BN, WM, WN, BKN> g;
  g.init(Ab, lda, Bb, ldb, smem);
  STAMP(KID, 0);
  static_for<0, NS - 1>([&](auto S) {
    if ((S.value & 1) == g.par) g.issue_all(S.value, S.value);
  });
  pre();
  if (g.par == 0) g.template wait_st<(NS - 2) / 2, 0>();
  BARRIER();
  STAMP(KID, 1);
  bf16x8 fa0[MT], fb0[NT], fa1[MT], fb1[NT];
  g.template rd_frag<0>(0, 0, fa0, fb0);
  constexpr int U = (NS % 2 == 0) ? NS : 2 * NS;
  constexpr int NSTEADY = T - NS + 1;
  constexpr int NBLK = NSTEADY / U;
#pragma unroll 1
  for (int tb = 0; tb < NBLK * U; tb += U) {
    static_for<0, U>([&](auto UU) {
      constexpr int u = UU.value;
      const int t = tb + u;
      g.template rd_frag<1>(u % NS, u % NS, fa1, fb1);
      __builtin_amdgcn_sched_barrier(0);
      g.template mma<false>(acc, fa0, fb0, 0, 0, false);
      __builtin_amdgcn_sched_barrier(0);
      if (g.par == ((u + 1) & 1)) g.template wait_st<(NS - 3) / 2, 0>();
      BARRIER();
      g.template rd_frag<0>((u + 1) % NS, (u + 1) % NS, fa0, fb0);
      __builtin_amdgcn_sched_barrier(0);
      g.template mma<true>(acc, fa1, fb1, t + NS - 1, (u + NS - 1) % NS, g.par == ((u + NS - 1) & 1));
      hook(t);
      __builtin_amdgcn_sched_barrier(0);
    });
  }
  static_for<NBLK * U, NSTEADY>([&](auto TT) {
    constexpr int t = TT.value;
    g.template rd_frag<1>(t % NS, t % NS, fa1, fb1);
    __builtin_amdgcn_sched_barrier(0);
    g.template mma<false>(acc, fa0, fb0, 0, 0, false);
    __builtin_amdgcn_sched_barrier(0);
    if (g.par == ((t + 1) & 1)) g.template wait_st<(NS - 3) / 2, 0>();
    BARRIER();
    g.template rd_frag<0>((t + 1) % NS, (t + 1) % NS, fa0, fb0);
    __builtin_amdgcn_sched_barrier(0);
    g.template mma<true>(acc, fa1, fb1, t + NS - 1, (t + NS - 1) % NS, g.par == ((t + NS - 1) & 1));
    hook(t);
    __builtin_amdgcn_sched_barrier(0);
  });
  static_for<T - NS + 1, T>([&](auto TT) {
    constexpr int t = TT.value;
    g.template rd_frag<1>(t % NS, t % NS, fa1, fb1);
    __builtin_amdgcn_sched_barrier(0);
    g.template mma<false>(acc, fa0, fb0, 0, 0, false);
    __builtin_amdgcn_sched_barrier(0);
    if constexpr (t + 1 < T) {
      constexpr int younger = (T - 1) >= t + 3 ? (T - 1 - (t + 1)) / 2 : 0;
      if (g.par == ((t + 1) & 1)) g.template wait_st<younger, 0>();
      BARRIER();
      g.template rd_frag<0>((t + 1) % NS, (t + 1) % NS, fa0, fb0);
      __builtin_amdgcn_sched_barrier(0);
    }
    g.template mma<false>(acc, fa1, fb1, 0, 0, false);
    hook(t);
    __builtin_amdgcn_sched_barrier(0);
  });
  STAMP(KID, 2);
}

struct NoHook {
  __device__ __forceinline__ void operator()(int) const {}
  __device__ __forceinline__ void operator()() const {}
};

__device__ __forceinline__ f32x2 ln_params(float mean, float var) {
  const float mb = rbf(mean), vb = rbf(var);
  const float sd = rbf(__fsqrt_rn(rbf(vb + 1e-5f)));
  const float ra = 1.f / sd;
  return f32x2{ra, -mb * ra};
}
__global__ __launch_bounds__(256) void k_pre(const u16* __restrict__ c, const u16* __restrict__ wmod,
                                             float* __restrict__ gates, float* __restrict__ tab,
                                             const u16* __restrict__ pw0, const u16* __restrict__ pw1,
                                             const u16* __restrict__ pw2, const u16* __restrict__ pw3,
                                             unsigned* __restrict__ cnt) {
  __shared__ float red[2][64][33];
  const int tid = threadIdx.x;
  if (blockIdx.x == 200) {
    for (int i = tid; i < 32 * 32; i += 256) cnt[i] = 0u;
  }
  if (blockIdx.x < 144) {
    const int cg = tid & 3, kq = tid >> 2;
    const int col0 = blockIdx.x * 32 + cg * 8;
    u32x4 w[12];
#pragma unroll
    for (int i = 0; i < 12; ++i) {
      int k = i * 64 + kq;
      w[i] = *(const u32x4*)(wmod + (size_t)k * (6 * DM) + col0);
    }
    float a0[8], a1[8];
#pragma unroll
    for (int j = 0; j < 8; ++j) a0[j] = a1[j] = 0.f;
#pragma unroll
    for (int i = 0; i < 12; ++i) {
      int k = i * 64 + kq;
      float c0 = bf2f(c[k]), c1 = bf2f(c[DM + k]);
      float s0 = rbf(c0 / (1.f + __expf(-c0)));
      float s1 = rbf(c1 / (1.f + __expf(-c1)));
#pragma unroll
      for (int j = 0; j < 4; ++j) {
        float wl = bflo(w[i][j]), wh = bfhi(w[i][j]);
        a0[2 * j] += s0 * wl; a0[2 * j + 1] += s0 * wh;
        a1[2 * j] += s1 * wl; a1[2 * j + 1] += s1 * wh;
      }
    }
#pragma unroll
    for (int j = 0; j < 8; ++j) {
      red[0][kq][cg * 8 + j] = a0[j];
      red[1][kq][cg * 8 + j] = a1[j];
    }
    __syncthreads();
    if (tid < 64) {
      int b = tid >> 5, cl = tid & 31;
      float s = 0.f;
      for (int q = 0; q < 64; ++q) s += red[b][q][cl];
      const float v = rbf(s);
      const int col = blockIdx.x * 32 + cl, i = col / DM, cc = col % DM;
      const int w_ = i / 3, kind = i % 3;
      if (kind == 2) gates[(b * 2 + w_) * DM + cc] = v;
      else if (kind == 1) tab[((b * 2 + w_) * 2 + 0) * DM + cc] = rbf(1.f + v);
      else tab[((b * 2 + w_) * 2 + 1) * DM + cc] = v;
    }
  } else {
    const int wb = blockIdx.x - 144;
    const u16* wp[4] = {pw0, pw1, pw2, pw3};
    const int nlines[4] = {DM * 3 * DM / 64, DM * DM / 64, DM * FF / 64, FF * DM / 64};
    unsigned accu = 0;
#pragma unroll
    for (int q = 0; q < 4; ++q) {
      for (int l = wb * 256 + tid; l < nlines[q]; l += 112 * 256) accu += *(const unsigned*)(wp[q] + (size_t)l * 64);
    }
    if (accu == 0x9e3779b9u && tid == 999) gates[0] = 0.f;
  }
}

__global__ __launch_bounds__(256) void k_lnmod(const u16* __restrict__ x, const float* __restrict__ tab, int which,
                                               u16* __restrict__ out) {
  const int lane = threadIdx.x & 63, row = blockIdx.x * 4 + (threadIdx.x >> 6);
  const int b = row >> 10;
  const u16* xr = x + (size_t)row * DM;
  float v[12];
#pragma unroll
  for (int i = 0; i < 3; ++i) {
    u32x2 u = *(const u32x2*)(xr + i * 256 + lane * 4);
    v[i * 4 + 0] = bflo(u[0]); v[i * 4 + 1] = bfhi(u[0]);
    v[i * 4 + 2] = bflo(u[1]); v[i * 4 + 3] = bfhi(u[1]);
  }
  const float* S = tab + (size_t)(b * 2 + which) * 2 * DM;
  const float* Tt = S + DM;
  f32x4 sc[3], sh[3];
#pragma unroll
  for (int i = 0; i < 3; ++i) {
    sc[i] = *(const f32x4*)(S + i * 256 + lane * 4);
    sh[i] = *(const f32x4*)(Tt + i * 256 + lane * 4);
  }
  float s = 0.f;
#pragma unroll
  for (int i = 0; i < 12; ++i) s += v[i];
#pragma unroll
  for (int o = 32; o; o >>= 1) s += __shfl_xor(s, o);
  const float mean = s * (1.f / DM);
  float q = 0.f;
#pragma unroll
  for (int i = 0; i < 12; ++i) { float d = v[i] - mean; q += d * d; }
#pragma unroll
  for (int o = 32; o; o >>= 1) q += __shfl_xor(q, o);
  const float mb = rbf(mean), vb = rbf(q * (1.f / DM));
  const float sd = rbf(__fsqrt_rn(rbf(vb + 1e-5f)));
#pragma unroll
  for (int i = 0; i < 3; ++i) {
    float h[4];
#pragma unroll
    for (int j = 0; j < 4; ++j) {
      float n = rbf(rbf(v[i * 4 + j] - mb) / sd);
      h[j] = rbf(n * sc[i][j]) + sh[i][j];
    }
    u32x2 o = {pack2(h[0], h[1]), pack2(h[2], h[3])};
    *(u32x2*)(out + (size_t)row * DM + i * 256 + lane * 4) = o;
  }
}

using GQKV = GC<128, 144, 4, 2, 4, true, false>;
__global__ __launch_bounds__(512, 2) void k_qkv(const u16* __restrict__ h, const u16* __restrict__ w,
                                                u16* __restrict__ qkv) {
  extern __shared__ __attribute__((aligned(16))) char smem[];
  using C = GQKV;
  constexpr int BM = 128, BN = 144;
  const int xc = blockIdx.x & 7, j = blockIdx.x >> 3;
  const int tm = (xc >> 2) * 8 + (j >> 2), tn = (xc & 3) * 4 + (j & 3);
  const int m0 = tm * BM, n0 = tn * BN;
  f32x4 acc[C::MT][C::NT] = {};
  gemm_main<BM, BN, 4, 2, 4, true, 12, 0>(h + (size_t)m0 * DM, DM, w + n0, 3 * DM, smem, acc, NoHook(), NoHook());
  const int lane = threadIdx.x & 63, wave = threadIdx.x >> 6, wm = wave % 4, wn = wave / 4;
#pragma unroll
  for (int m = 0; m < C::MT; ++m)
#pragma unroll
    for (int n = 0; n < C::NT; ++n) {
      if (wn * C::NT + n < C::NTT) {
        int row = m0 + wm * C::WTM + m * 16 + (lane & 15);
        int col = n0 + wn * C::WTN + n * 16 + (lane >> 4) * 4;
        u32x2 v = {pack2(acc[m][n][0], acc[m][n][1]), pack2(acc[m][n][2], acc[m][n][3])};
        *(u32x2*)(qkv + (size_t)row * (3 * DM) + col) = v;
      }
    }
  STAMP(0, 3);
}

using GSC = GC<128, 128, 4, 2, 4, false, false>;
__global__ __launch_bounds__(512, 2) void k_scores(const u16* __restrict__ qkv, u16* __restrict__ E,
                                                   f32x2* __restrict__ st) {
  extern __shared__ __attribute__((aligned(16))) char smem[];
  using C = GSC;
  constexpr int BM = 128, BN = 128;
  const int xc = blockIdx.x & 7, j = blockIdx.x >> 3;
  const int bh = xc >> 1, b = bh >> 1, hh = bh & 1;
  const int tm = (xc & 1) * 4 + (j >> 3), tn = j & 7;
  const int m0 = tm * BM, n0 = tn * BN;
  const u16* Q = qkv + (size_t)(b * NSEQ + m0) * (3 * DM) + hh * DH;
  const u16* K = qkv + (size_t)(b * NSEQ + n0) * (3 * DM) + DM + hh * DH;
  f32x4 acc[C::MT][C::NT] = {};
  gemm_main<BM, BN, 4, 2, 4, false, 6, 1>(Q, 3 * DM, K, 3 * DM, smem, acc, NoHook(), NoHook());
  const int lane = threadIdx.x & 63, wave = threadIdx.x >> 6, wm = wave % 4, wn = wave / 4;
  const float scale = 0.05103103630798288f;
#pragma unroll
  for (int m = 0; m < C::MT; ++m) {
    float s[C::NT][4];
    float mx = -INFINITY;
#pragma unroll
    for (int n = 0; n < C::NT; ++n) {
      unsigned p0 = pack2(acc[m][n][0], acc[m][n][1]), p1 = pack2(acc[m][n][2], acc[m][n][3]);
      p0 = pack2(bflo(p0) * scale, bfhi(p0) * scale);
      p1 = pack2(bflo(p1) * scale, bfhi(p1) * scale);
      s[n][0] = bflo(p0); s[n][1] = bfhi(p0); s[n][2] = bflo(p1); s[n][3] = bfhi(p1);
      mx = fmaxf(mx, fmaxf(fmaxf(s[n][0], s[n][1]), fmaxf(s[n][2], s[n][3])));
    }
    mx = fmaxf(mx, __shfl_xor(mx, 16));
    mx = fmaxf(mx, __shfl_xor(mx, 32));
    float sum = 0.f;
    const int row = m0 + wm * C::WTM + m * 16 + (lane & 15);
    u16* Er = E + ((size_t)bh * NSEQ + row) * NSEQ + n0 + wn * C::WTN + (lane >> 4) * 4;
#pragma unroll
    for (int n = 0; n < C::NT; ++n) {
#pragma unroll
      for (int e = 0; e < 4; ++e) {
        s[n][e] = __expf(s[n][e] - mx);
        sum += s[n][e];
      }
      u32x2 v = {pack2(s[n][0], s[n][1]), pack2(s[n][2], s[n][3])};
      *(u32x2*)(Er + n * 16) = v;
    }
    sum += __shfl_xor(sum, 16);
    sum += __shfl_xor(sum, 32);
    if (lane < 16) st[((size_t)bh * NSEQ + row) * 16 + tn * 2 + wn] = f32x2{mx, sum};
  }
  STAMP(1, 3);
}

using GPV = GC<64, 96, 4, 2, 6, true, false>;
__global__ __launch_bounds__(512, 2) void k_pv(const u16* __restrict__ E, const f32x2* __restrict__ st,
                                               const u16* __restrict__ qkv, u16* __restrict__ O) {
  extern __shared__ __attribute__((aligned(16))) char smem[];
  using C = GPV;
  constexpr int BM = 64, BN = 96;
  const int xc = blockIdx.x & 7, j = blockIdx.x >> 3;
  const int bh = xc >> 1, b = bh >> 1, hh = bh & 1;
  const int tm = (xc & 1) * 8 + (j >> 2), tn = j & 3;
  const int m0 = tm * BM, n0 = tn * BN;
  const int lane = threadIdx.x & 63, wave = threadIdx.x >> 6, wm = wave % 4, wn = wave / 4;
  float* ftab = (float*)(smem + C::LDS);
  auto pre = [&]() {
    if (threadIdx.x < 64) {
      const f32x4* p = (const f32x4*)(st + ((size_t)bh * NSEQ + m0 + threadIdx.x) * 16);
      f32x4 v[8];
#pragma unroll
      for (int t = 0; t < 8; ++t) v[t] = p[t];
      float M = -INFINITY;
#pragma unroll
      for (int t = 0; t < 8; ++t) M = fmaxf(M, fmaxf(v[t][0], v[t][2]));
      float fac[16], Ls = 0.f;
#pragma unroll
      for (int t = 0; t < 8; ++t) {
        fac[2 * t] = __expf(v[t][0] - M);
        fac[2 * t + 1] = __expf(v[t][2] - M);
        Ls += fac[2 * t] * v[t][1] + fac[2 * t + 1] * v[t][3];
      }
      float inv = 1.f / Ls;
#pragma unroll
      for (int t = 0; t < 16; ++t) ftab[t * 64 + threadIdx.x] = fac[t] * inv;
      asm volatile("s_waitcnt lgkmcnt(0)" ::: "memory");
    }
  };
  const u16* A = E + ((size_t)bh * NSEQ + m0) * NSEQ;
  const u16* V = qkv + (size_t)(b * NSEQ) * (3 * DM) + 2 * DM + hh * DH + n0;
  f32x4 acc[1][3] = {};
  f32x4 tot[1][3] = {};
  const float* frow = ftab + wm * 16 + (lane & 15);
  auto hook = [&](int t) {
    const float f = frow[t * 64];
#pragma unroll
    for (int n = 0; n < 3; ++n) {
      tot[0][n] += acc[0][n] * f;
      acc[0][n] = f32x4{0.f, 0.f, 0.f, 0.f};
    }
  };
  gemm_main<BM, BN, 4, 2, 6, true, 16, 2>(A, NSEQ, V, 3 * DM, smem, acc, pre, hook);
#pragma unroll
  for (int n = 0; n < 3; ++n) {
    int row = b * NSEQ + m0 + wm * 16 + (lane & 15);
    int col = hh * DH + n0 + wn * C::WTN + n * 16 + (lane >> 4) * 4;
    u32x2 v = {pack2(tot[0][n][0], tot[0][n][1]), pack2(tot[0][n][2], tot[0][n][3])};
    *(u32x2*)(O + (size_t)row * DM + col) = v;
  }
  STAMP(2, 3);
}

template <int KI, int LDA, bool STATS>
__device__ __forceinline__ void res_gemm(const u16* __restrict__ A, const u16* __restrict__ W,
                                         const u16* __restrict__ xin, const float* __restrict__ gates,
                                         u16* __restrict__ xo, unsigned long long* pst, unsigned* cnt,
                                         const float* __restrict__ tab, u16* __restrict__ h2, char* smem) {
  using C = GPV;
  constexpr int BM = 64, BN = 96;
  const int xc = blockIdx.x & 7, j = blockIdx.x >> 3;
  const int tm = (xc >> 1) * 8 + (j >> 2), tn = (xc & 1) * 4 + (j & 3);
  const int m0 = tm * BM, n0 = tn * BN, b = m0 >> 10;
  const int lane = threadIdx.x & 63, wave = threadIdx.x >> 6, wm = wave % 4, wn = wave / 4;
  const int row = m0 + wm * 16 + (lane & 15);
  const int col0 = n0 + wn * C::WTN + (lane >> 4) * 4;
  f32x4 acc[1][3] = {};
  gemm_main<BM, BN, 4, 2, 6, true, LDA / 64, KI>(A + (size_t)m0 * LDA, LDA, W + n0, DM, smem, acc, NoHook(), NoHook());
  float vals[12];
#pragma unroll
  for (int n = 0; n < 3; ++n) {
    u32x2 xv = *(const u32x2*)(xin + (size_t)row * DM + col0 + n * 16);
    f32x4 gt = *(const f32x4*)(gates + b * (2 * DM) + col0 + n * 16);
    unsigned a0 = pack2(acc[0][n][0], acc[0][n][1]), a1 = pack2(acc[0][n][2], acc[0][n][3]);
    unsigned g0 = pack2(gt[0] * bflo(a0), gt[1] * bfhi(a0));
    unsigned g1 = pack2(gt[2] * bflo(a1), gt[3] * bfhi(a1));
    u32x2 o = {pack2(bflo(xv[0]) + bflo(g0), bfhi(xv[0]) + bfhi(g0)),
               pack2(bflo(xv[1]) + bflo(g1), bfhi(xv[1]) + bfhi(g1))};
    *(u32x2*)(xo + (size_t)row * DM + col0 + n * 16) = o;
    vals[n * 4 + 0] = bflo(o[0]); vals[n * 4 + 1] = bfhi(o[0]);
    vals[n * 4 + 2] = bflo(o[1]); vals[n * 4 + 3] = bfhi(o[1]);
  }
  if (STATS) {
    float s = 0.f;
#pragma unroll
    for (int i = 0; i < 12; ++i) s += vals[i];
    s += __shfl_xor(s, 16);
    s += __shfl_xor(s, 32);
    const float lm = s * (1.f / 48.f);
    float q = 0.f;
#pragma unroll
    for (int i = 0; i < 12; ++i) { float d = vals[i] - lm; q += d * d; }
    q += __shfl_xor(q, 16);
    q += __shfl_xor(q, 32);
    if (lane < 16)
      __hip_atomic_store(pst + (size_t)row * 16 + tn * 2 + wn,
                         ((unsigned long long)__float_as_uint(q) << 32) | __float_as_uint(s), __ATOMIC_RELAXED,
                         __HIP_MEMORY_SCOPE_AGENT);
    f32x4 sc[3], sh[3];
#pragma unroll
    for (int n = 0; n < 3; ++n) {
      sc[n] = *(const f32x4*)(tab + (size_t)(b * 2 + 1) * 2 * DM + col0 + n * 16);
      sh[n] = *(const f32x4*)(tab + (size_t)(b * 2 + 1) * 2 * DM + DM + col0 + n * 16);
    }
    asm volatile("s_waitcnt vmcnt(0)" ::: "memory");
    __syncthreads();
    if (threadIdx.x == 0) {
      unsigned* cw = cnt + tm * 32;
      __hip_atomic_fetch_add(cw, 1u, __ATOMIC_RELAXED, __HIP_MEMORY_SCOPE_AGENT);
      for (unsigned spins = 0; spins < (1u << 20); ++spins) {
        if (__hip_atomic_load(cw, __ATOMIC_RELAXED, __HIP_MEMORY_SCOPE_AGENT) >= 8u) break;
        __builtin_amdgcn_s_sleep(1);
      }
    }
    __syncthreads();
    float ps[16], pq[16];
#pragma unroll
    for (int i = 0; i < 16; ++i) {
      unsigned long long v =
          __hip_atomic_load(pst + (size_t)row * 16 + i, __ATOMIC_RELAXED, __HIP_MEMORY_SCOPE_AGENT);
      ps[i] = __uint_as_float((unsigned)v);
      pq[i] = __uint_as_float((unsigned)(v >> 32));
    }
    float tot = 0.f;
#pragma unroll
    for (int i = 0; i < 16; ++i) tot += ps[i];
    const float mean = tot * (1.f / DM);
    float m2 = 0.f;
#pragma unroll
    for (int i = 0; i < 16; ++i) {
      float d = ps[i] * (1.f / 48.f) - mean;
      m2 += pq[i] + 48.f * d * d;
    }
    const float mb = rbf(mean), vb = rbf(m2 * (1.f / DM));
    const float sd = rbf(__fsqrt_rn(rbf(vb + 1e-5f)));
#pragma unroll
    for (int n = 0; n < 3; ++n) {
      float h[4];
#pragma unroll
      for (int e = 0; e < 4; ++e) {
        float nn = rbf(rbf(vals[n * 4 + e] - mb) / sd);
        h[e] = rbf(nn * sc[n][e]) + sh[n][e];
      }
      u32x2 o = {pack2(h[0], h[1]), pack2(h[2], h[3])};
      *(u32x2*)(h2 + (size_t)row * DM + col0 + n * 16) = o;
    }
  }
  STAMP(KI, 3);
}
__global__ __launch_bounds__(512, 2) void k_wo(const u16* __restrict__ O, const u16* __restrict__ wo,
                                               const u16* __restrict__ xin, const float* __restrict__ gates,
                                               u16* __restrict__ x1, unsigned long long* pst, unsigned* cnt,
                                               const float* __restrict__ tab, u16* __restrict__ h2) {
  extern __shared__ __attribute__((aligned(16))) char smem[];
  res_gemm<3, DM, true>(O, wo, xin, gates, x1, pst, cnt, tab, h2, smem);
}
__global__ __launch_bounds__(512, 2) void k_fc2(const u16* __restrict__ f, const u16* __restrict__ w2,
                                                const u16* __restrict__ x1, const float* __restrict__ gates,
                                                u16* __restrict__ out) {
  extern __shared__ __attribute__((aligned(16))) char smem[];
  res_gemm<5, FF, false>(f, w2, x1, gates + DM, out, nullptr, nullptr, nullptr, nullptr, smem);
}

__device__ __forceinline__ float gelu_tanh(float u) {
  float w = u * (-2.f * 0.7978845608028654f * 1.4426950408889634f);
  float a = w * (1.f + 0.044715f * u * u);
  return u * __builtin_amdgcn_rcpf(1.f + __builtin_amdgcn_exp2f(a));
}
using GFC = GC<128, 192, 2, 4, 4, true, false>;
__global__ __launch_bounds__(512, 2) void k_fc1(const u16* __restrict__ h2, const u16* __restrict__ w1,
                                                u16* __restrict__ f) {
  extern __shared__ __attribute__((aligned(16))) char smem[];
  using C = GFC;
  constexpr int BM = 128, BN = 192;
  const int xc = blockIdx.x & 7, j = blockIdx.x >> 3;
  const int tm = (xc >> 2) * 8 + (j >> 2), tn = (xc & 3) * 4 + (j & 3);
  const int m0 = tm * BM, n0 = tn * BN;
  f32x4 acc[C::MT][C::NT] = {};
  gemm_main<BM, BN, 2, 4, 4, true, 12, 4>(h2 + (size_t)m0 * DM, DM, w1 + n0, FF, smem, acc, NoHook(), NoHook());
  const int lane = threadIdx.x & 63, wave = threadIdx.x >> 6, wm = wave % 2, wn = wave / 2;
#pragma unroll
  for (int m = 0; m < C::MT; ++m)
#pragma unroll
    for (int n = 0; n < C::NT; ++n) {
      int row = m0 + wm * C::WTM + m * 16 + (lane & 15);
      int col = n0 + wn * C::WTN + n * 16 + (lane >> 4) * 4;
      unsigned p0 = pack2(acc[m][n][0], acc[m][n][1]), p1 = pack2(acc[m][n][2], acc[m][n][3]);
      u32x2 v = {pack2(gelu_tanh(bflo(p0)), gelu_tanh(bfhi(p0))), pack2(gelu_tanh(bflo(p1)), gelu_tanh(bfhi(p1)))};
      *(u32x2*)(f + (size_t)row * FF + col) = v;
    }
  STAMP(4, 3);
}

extern "C" void kernel_launch(void* const* d_in, const int* in_sizes, int n_in, void* d_out, int out_size,
                              void* d_ws, size_t ws_size, hipStream_t stream) {
  const u16* x = (const u16*)d_in[0];
  const u16* c = (const u16*)d_in[1];
  const u16* wqkv = (const u16*)d_in[2];
  const u16* wo = (const u16*)d_in[3];
  const u16* wmod = (const u16*)d_in[4];
  const u16* w1 = (const u16*)d_in[5];
  const u16* w2 = (const u16*)d_in[6];
  u16* out = (u16*)d_out;

  char* ws = (char*)d_ws;
  size_t o = 0;
  auto take = [&](size_t bytes) { char* p = ws + o; o += (bytes + 255) & ~(size_t)255; return p; };
  float* gates = (float*)take(2 * 2 * DM * 4);
  float* tab = (float*)take(2 * 2 * 2 * DM * 4);
  u16* h = (u16*)take((size_t)MROWS * DM * 2);
  u16* h2 = (u16*)take((size_t)MROWS * DM * 2);
  u16* qkv = (u16*)take((size_t)MROWS * 3 * DM * 2);
  u16* E = (u16*)take((size_t)4 * NSEQ * NSEQ * 2);
  f32x2* st = (f32x2*)take((size_t)4 * NSEQ * 16 * 8);
  u16* O = (u16*)take((size_t)MROWS * DM * 2);
  u16* x1 = (u16*)take((size_t)MROWS * DM * 2);
  unsigned long long* pst = (unsigned long long*)take((size_t)MROWS * 16 * 8);
  unsigned* cnt = (unsigned*)take(32 * 32 * 4);
  u16* f = (u16*)take((size_t)MROWS * FF * 2);

  constexpr int LDS_QKV = GQKV::LDS;
  constexpr int LDS_SC = GSC::LDS;
  constexpr int LDS_PV = GPV::LDS + 16 * 64 * 4;
  constexpr int LDS_WO = GPV::LDS;
  constexpr int LDS_FC = GFC::LDS;
  (void)hipFuncSetAttribute((const void*)k_qkv, hipFuncAttributeMaxDynamicSharedMemorySize, LDS_QKV);
  (void)hipFuncSetAttribute((const void*)k_scores, hipFuncAttributeMaxDynamicSharedMemorySize, LDS_SC);
  (void)hipFuncSetAttribute((const void*)k_pv, hipFuncAttributeMaxDynamicSharedMemorySize, LDS_PV);
  (void)hipFuncSetAttribute((const void*)k_wo, hipFuncAttributeMaxDynamicSharedMemorySize, LDS_WO);
  (void)hipFuncSetAttribute((const void*)k_fc1, hipFuncAttributeMaxDynamicSharedMemorySize, LDS_FC);
  (void)hipFuncSetAttribute((const void*)k_fc2, hipFuncAttributeMaxDynamicSharedMemorySize, LDS_WO);

  k_pre<<<144 + 112, 256, 0, stream>>>(c, wmod, gates, tab, wqkv, wo, w1, w2, cnt);
  k_lnmod<<<MROWS / 4, 256, 0, stream>>>(x, tab, 0, h);
  k_qkv<<<256, 512, LDS_QKV, stream>>>(h, wqkv, qkv);
  k_scores<<<256, 512, LDS_SC, stream>>>(qkv, E, st);
  k_pv<<<256, 512, LDS_PV, stream>>>(E, st, qkv, O);
  k_wo<<<256, 512, LDS_WO, stream>>>(O, wo, x, gates, x1, pst, cnt, tab, h2);
  k_fc1<<<256, 512, LDS_FC, stream>>>(h2, w1, f);
  k_fc2<<<256, 512, LDS_WO, stream>>>(f, w2, x1, gates, out);
}
```

```cpp
#include <hip/hip_runtime.h>
#include <hip/hip_bf16.h>
#include <stdint.h>

typedef unsigned short u16;
typedef __attribute__((ext_vector_type(8))) __bf16 bf16x8;
typedef __attribute__((ext_vector_type(4))) __bf16 bf16x4;
typedef __attribute__((ext_vector_type(2))) __bf16 bf16x2;
typedef __attribute__((ext_vector_type(4))) float f32x4;
typedef __attribute__((ext_vector_type(2))) float f32x2;
typedef __attribute__((ext_vector_type(4))) unsigned u32x4;
typedef __attribute__((ext_vector_type(2))) unsigned u32x2;
#define LAS __attribute__((address_space(3)))
#define GAS __attribute__((address_space(1)))

constexpr int NB = 2, NSEQ = 1024, DM = 768, NH = 2, DH = 384, FF = 3072;
constexpr int MROWS = NB * NSEQ;

__device__ __forceinline__ float bf2f(u16 v) { return __uint_as_float(((unsigned)v) << 16); }
__device__ __forceinline__ float bflo(unsigned v) { return __uint_as_float(v << 16); }
__device__ __forceinline__ float bfhi(unsigned v) { return __uint_as_float(v & 0xffff0000u); }
__device__ __forceinline__ unsigned pack2(float a, float b) {
  f32x2 f = {a, b};
  bf16x2 r = __builtin_convertvector(f, bf16x2);
  return __builtin_bit_cast(unsigned, r);
}
__device__ __forceinline__ float rbf(float a) {
  return bflo(pack2(a, 0.f) & 0xffffu);
}

template <int N>
__device__ __forceinline__ void waitv() {
  asm volatile("s_waitcnt vmcnt(%0)" ::"i"(N) : "memory");
}
template <int I>
struct IC {
  static constexpr int value = I;
};
template <int B, int E, class F>
__device__ __forceinline__ void static_for(F&& f) {
  if constexpr (B < E) {
    f(IC<B>{});
    static_for<B + 1, E>(f);
  }
}
#define ABL 0
#define FENCE() asm volatile("" ::: "memory")
#define STAMP(kid, i)
#define BARRIER()                        \
  do {                                   \
    FENCE();                             \
    __builtin_amdgcn_s_barrier();        \
    FENCE();                             \
  } while (0)

template <int BM, int BN, int WM, int WN, int NS, bool BKN, bool ATR>
struct GC {
  static constexpr int NW = WM * WN;
  static constexpr int WTM = BM / WM, MT = WTM / 16;
  static constexpr int NTT = BN / 16;
  static constexpr int NT = (NTT + WN - 1) / WN;
  static constexpr int WTN = NT * 16;
  static constexpr int A_BYTES = BM * 128, B_BYTES = BN * 128;
  static constexpr int NSA = ATR ? 3 : NS;
  static constexpr int B_OFF = NSA * A_BYTES;
  static constexpr int TAB_OFF = B_OFF + NS * B_BYTES;
  static constexpr int NA = BM / 8, NBI = BN / 8;
  static constexpr int LA = ATR ? 0 : NA / 4;
  static constexpr int LB = (NBI + 3) / 4;
  static constexpr int NFULL = NBI % 4 == 0 ? 4 : NBI % 4;
  static constexpr int L = LA + LB;
  static constexpr int LDS = TAB_OFF + (ATR ? 8192 : 0);
  static_assert(NW == 8, "8 waves");
  static_assert(NA % 4 == 0, "A instr split");
  static_assert(NS >= 4, "stages");
  static_assert(LDS <= 163840, "lds");
};

template <class C, int BN, int WM, int WN, bool BKN>
struct GemmCtx {
  static constexpr int MT = C::MT, NT = C::NT, L = C::L, LA = C::LA, LB = C::LB;
  const char* Abase;
  const char* Bbase;
  size_t bstep;
  char* smem;
  unsigned goff[L > 0 ? L : 1];
  unsigned aoff[2], boff[2];
  int wq, par, last_n;
  bool full;

  __device__ __forceinline__ void init(const u16* Ab, int lda, const u16* Bb, int ldb, char* sm) {
    const int tid = threadIdx.x, lane = tid & 63;
    const int wave = __builtin_amdgcn_readfirstlane(tid >> 6);
    const int wm = wave % WM, wn = wave / WM;
    wq = wave & 3;
    par = wave >> 2;
    full = (C::NFULL == 4) || (wq < C::NFULL);
    smem = sm;
#pragma unroll
    for (int i = 0; i < LA; ++i) {
      int r = i * 4 + wq;
      int row = r * 8 + (lane >> 3);
      int lc = (lane & 7) ^ (row & 7);
      goff[i] = (unsigned)(row * lda + lc * 8) * 2u;
    }
#pragma unroll
    for (int i = 0; i < LB; ++i) {
      int r = i * 4 + wq;
      if (r >= C::NBI) r = 0;
      if (BKN) {
        int st = r * 4 + (lane >> 4);
        int kb = st / (BN / 16), nb = st % (BN / 16);
        int i16 = lane & 15, prow = i16 >> 1, hf = i16 & 1;
        int lrow = prow ^ ((kb & 1) << 2);
        goff[LA + i] = (unsigned)((kb * 8 + lrow) * ldb + nb * 16 + hf * 8) * 2u;
      } else {
        int row = r * 8 + (lane >> 3);
        int lc = (lane & 7) ^ (row & 7);
        goff[LA + i] = (unsigned)(row * ldb + lc * 8) * 2u;
      }
    }
    Abase = (const char*)Ab;
    Bbase = (const char*)Bb;
    bstep = BKN ? (size_t)64 * ldb * 2 : (size_t)128;
    const int g = lane >> 4, i16 = lane & 15;
#pragma unroll
    for (int s = 0; s < 2; ++s)
      aoff[s] = (unsigned)((wm * C::WTM + i16) * 128 + (((s * 4 + g) ^ (lane & 7)) << 4));
    if (BKN) {
      const int q4 = i16 >> 2, p = i16 & 3;
#pragma unroll
      for (int h = 0; h < 2; ++h)
        boff[h] = (unsigned)(C::B_OFF + (g * (BN / 16) + wn * NT) * 256 + (((h * 4 + q4) ^ ((g & 1) << 2)) * 32) +
                             p * 8);
    } else {
#pragma unroll
      for (int s = 0; s < 2; ++s)
        boff[s] = (unsigned)(C::B_OFF + (wn * C::WTN + i16) * 128 + (((s * 4 + g) ^ (lane & 7)) << 4));
    }
    const bool last_dummy = (C::NTT % WN != 0) && (wn * NT + NT > C::NTT);
    last_n = last_dummy ? 0 : NT - 1;
  }
  template <int i>
  __device__ __forceinline__ void issue1(int t, int sl) {
    if constexpr (i < LA) {
      __builtin_amdgcn_global_load_lds((const GAS unsigned*)(Abase + (size_t)t * 128 + goff[i]),
                                       (LAS unsigned*)(smem + sl * C::A_BYTES + wq * 1024 + i * 4096), 16, 0, 0);
    } else {
      if (i + 1 < L || full)
        __builtin_amdgcn_global_load_lds(
            (const GAS unsigned*)(Bbase + (size_t)t * bstep + goff[i]),
            (LAS unsigned*)(smem + C::B_OFF + sl * C::B_BYTES + wq * 1024 + (i - LA) * 4096), 16, 0, 0);
    }
  }
  __device__ __forceinline__ void issue_all(int t, int sl) {
    static_for<0, L>([&](auto II) { issue1<II.value>(t, sl); });
  }
  template <int n, int x>
  __device__ __forceinline__ void wait_st() {
    if (C::NFULL == 4 || n == 0) {
      waitv<n * L + x>();
    } else {
      if (full) waitv<n * L + x>();
      else waitv<n * (L - 1) + x>();
    }
  }
  template <int s>
  __device__ __forceinline__ void rd_frag(int sla, int slb, bf16x8 (&fa)[MT], bf16x8 (&fb)[NT]) {
    const char* sa = smem + sla * C::A_BYTES;
    const char* sb = smem + slb * C::B_BYTES;
#pragma unroll
    for (int m = 0; m < MT; ++m) fa[m] = *(const bf16x8*)(sa + aoff[s] + m * 2048);
#pragma unroll
    for (int n = 0; n < NT; ++n) {
      const int nn = (n == NT - 1) ? last_n : n;
      if (BKN) {
        bf16x4 lo = __builtin_amdgcn_ds_read_tr16_b64_v4bf16(
            (LAS bf16x4*)(sb + boff[0] + s * 4 * (BN / 16) * 256 + nn * 256));
        bf16x4 hi = __builtin_amdgcn_ds_read_tr16_b64_v4bf16(
            (LAS bf16x4*)(sb + boff[1] + s * 4 * (BN / 16) * 256 + nn * 256));
        fb[n] = __builtin_shufflevector(lo, hi, 0, 1, 2, 3, 4, 5, 6, 7);
      } else {
        fb[n] = *(const bf16x8*)(sb + boff[s] + nn * 2048);
      }
    }
  }
  template <bool ISS>
  __device__ __forceinline__ void mma(f32x4 (&acc)[MT][NT], bf16x8 (&fa)[MT], bf16x8 (&fb)[NT], int ti, int sl,
                                      bool do_issue) {
    static_for<0, MT * NT>([&](auto Q) {
      constexpr int q = Q.value, m = q / NT, n = q % NT;
      acc[m][n] = __builtin_amdgcn_mfma_f32_16x16x32_bf16(fb[n], fa[m], acc[m][n], 0, 0, 0);
      if constexpr (ISS) {
        constexpr int lo = (q * L) / (MT * NT), hi = ((q + 1) * L) / (MT * NT);
        if constexpr (hi > lo) {
          if (do_issue) static_for<lo, hi>([&](auto II) { issue1<II.value>(ti, sl); });
          __builtin_amdgcn_sched_barrier(0);
        }
      }
    });
  }
};

template <int BM, int BN, int WM, int WN, int NS, bool BKN, int T, int KID, class Pre, class Hook>
__device__ __forceinline__ void gemm_main(const u16* __restrict__ Ab, int lda, const u16* __restrict__ Bb, int ldb,
                                          char* smem,
                                          f32x4 (&acc)[GC<BM, BN, WM, WN, NS, BKN, false>::MT][GC<BM, BN, WM, WN, NS, BKN, false>::NT],
                                          Pre pre, Hook hook) {
  using C = GC<BM, BN, WM, WN, NS, BKN, false>;
  constexpr int MT = C::MT, NT = C::NT;
  GemmCtx<C, BN, WM, WN, BKN> g;
  g.init(Ab, lda, Bb, ldb, smem);
  STAMP(KID, 0);
  static_for<0, NS - 1>([&](auto S) {
    if ((S.value & 1) == g.par) g.issue_all(S.value, S.value);
  });
  pre();
  if (g.par == 0) g.template wait_st<(NS - 2) / 2, 0>();
  BARRIER();
  STAMP(KID, 1);
  bf16x8 fa0[MT], fb0[NT], fa1[MT], fb1[NT];
  g.template rd_frag<0>(0, 0, fa0, fb0);
  constexpr int U = (NS % 2 == 0) ? NS : 2 * NS;
  constexpr int NSTEADY = T - NS + 1;
  constexpr int NBLK = NSTEADY / U;
#pragma unroll 1
  for (int tb = 0; tb < NBLK * U; tb += U) {
    static_for<0, U>([&](auto UU) {
      constexpr int u = UU.value;
      const int t = tb + u;
      g.template rd_frag<1>(u % NS, u % NS, fa1, fb1);
      __builtin_amdgcn_sched_barrier(0);
      g.template mma<false>(acc, fa0, fb0, 0, 0, false);
      __builtin_amdgcn_sched_barrier(0);
      if (g.par == ((u + 1) & 1)) g.template wait_st<(NS - 3) / 2, 0>();
      BARRIER();
      g.template rd_frag<0>((u + 1) % NS, (u + 1) % NS, fa0, fb0);
      __builtin_amdgcn_sched_barrier(0);
      g.template mma<true>(acc, fa1, fb1, t + NS - 1, (u + NS - 1) % NS, g.par == ((u + NS - 1) & 1));
      hook(t);
      __builtin_amdgcn_sched_barrier(0);
    });
  }
  static_for<NBLK * U, NSTEADY>([&](auto TT) {
    constexpr int t = TT.value;
    g.template rd_frag<1>(t % NS, t % NS, fa1, fb1);
    __builtin_amdgcn_sched_barrier(0);
    g.template mma<false>(acc, fa0, fb0, 0, 0, false);
    __builtin_amdgcn_sched_barrier(0);
    if (g.par == ((t + 1) & 1)) g.template wait_st<(NS - 3) / 2, 0>();
    BARRIER();
    g.template rd_frag<0>((t + 1) % NS, (t + 1) % NS, fa0, fb0);
    __builtin_amdgcn_sched_barrier(0);
    g.template mma<true>(acc, fa1, fb1, t + NS - 1, (t + NS - 1) % NS, g.par == ((t + NS - 1) & 1));
    hook(t);
    __builtin_amdgcn_sched_barrier(0);
  });
  static_for<T - NS + 1, T>([&](auto TT) {
    constexpr int t = TT.value;
    g.template rd_frag<1>(t % NS, t % NS, fa1, fb1);
    __builtin_amdgcn_sched_barrier(0);
    g.template mma<false>(acc, fa0, fb0, 0, 0, false);
    __builtin_amdgcn_sched_barrier(0);
    if constexpr (t + 1 < T) {
      constexpr int younger = (T - 1) >= t + 3 ? (T - 1 - (t + 1)) / 2 : 0;
      if (g.par == ((t + 1) & 1)) g.template wait_st<younger, 0>();
      BARRIER();
      g.template rd_frag<0>((t + 1) % NS, (t + 1) % NS, fa0, fb0);
      __builtin_amdgcn_sched_barrier(0);
    }
    g.template mma<false>(acc, fa1, fb1, 0, 0, false);
    hook(t);
    __builtin_amdgcn_sched_barrier(0);
  });
  STAMP(KID, 2);
}

__device__ __forceinline__ void store_pair(u16* rowp, u32x2 a, u32x2 b) {
  auto rx = __builtin_amdgcn_permlane16_swap(a[0], b[0], false, false);
  auto ry = __builtin_amdgcn_permlane16_swap(a[1], b[1], false, false);
  const int g = (threadIdx.x & 63) >> 4;
  u32x4 v = {rx[0], ry[0], rx[1], ry[1]};
  *(u32x4*)(rowp + (g & 1) * 16 + (g >> 1) * 8) = v;
}

struct NoHook {
  __device__ __forceinline__ void operator()(int) const {}
  __device__ __forceinline__ void operator()() const {}
};

__device__ __forceinline__ f32x2 ln_params(float mean, float var) {
  const float mb = rbf(mean), vb = rbf(var);
  const float sd = rbf(__fsqrt_rn(rbf(vb + 1e-5f)));
  const float ra = 1.f / sd;
  return f32x2{ra, -mb * ra};
}
__global__ __launch_bounds__(256) void k_pre(const u16* __restrict__ c, const u16* __restrict__ wmod,
                                             float* __restrict__ gates, float* __restrict__ tab,
                                             const u16* __restrict__ pw0, const u16* __restrict__ pw1,
                                             const u16* __restrict__ pw2, const u16* __restrict__ pw3) {
  __shared__ float red[2][64][33];
  const int tid = threadIdx.x;
  if (blockIdx.x < 144) {
    const int cg = tid & 3, kq = tid >> 2;
    const int col0 = blockIdx.x * 32 + cg * 8;
    u32x4 w[12];
#pragma unroll
    for (int i = 0; i < 12; ++i) {
      int k = i * 64 + kq;
      w[i] = *(const u32x4*)(wmod + (size_t)k * (6 * DM) + col0);
    }
    float a0[8], a1[8];
#pragma unroll
    for (int j = 0; j < 8; ++j) a0[j] = a1[j] = 0.f;
#pragma unroll
    for (int i = 0; i < 12; ++i) {
      int k = i * 64 + kq;
      float c0 = bf2f(c[k]), c1 = bf2f(c[DM + k]);
      float s0 = rbf(c0 / (1.f + __expf(-c0)));
      float s1 = rbf(c1 / (1.f + __expf(-c1)));
#pragma unroll
      for (int j = 0; j < 4; ++j) {
        float wl = bflo(w[i][j]), wh = bfhi(w[i][j]);
        a0[2 * j] += s0 * wl; a0[2 * j + 1] += s0 * wh;
        a1[2 * j] += s1 * wl; a1[2 * j + 1] += s1 * wh;
      }
    }
#pragma unroll
    for (int j = 0; j < 8; ++j) {
      red[0][kq][cg * 8 + j] = a0[j];
      red[1][kq][cg * 8 + j] = a1[j];
    }
    __syncthreads();
    if (tid < 64) {
      int b = tid >> 5, cl = tid & 31;
      float s = 0.f;
      for (int q = 0; q < 64; ++q) s += red[b][q][cl];
      const float v = rbf(s);
      const int col = blockIdx.x * 32 + cl, i = col / DM, cc = col % DM;
      const int w_ = i / 3, kind = i % 3;
      if (kind == 2) gates[(b * 2 + w_) * DM + cc] = v;
      else if (kind == 1) tab[((b * 2 + w_) * 2 + 0) * DM + cc] = rbf(1.f + v);
      else tab[((b * 2 + w_) * 2 + 1) * DM + cc] = v;
    }
  } else {
    const int wb = blockIdx.x - 144;
    const u16* wp[4] = {pw0, pw1, pw2, pw3};
    const int nlines[4] = {DM * 3 * DM / 64, DM * DM / 64, DM * FF / 64, FF * DM / 64};
    unsigned accu = 0;
#pragma unroll
    for (int q = 0; q < 4; ++q) {
      for (int l = wb * 256 + tid; l < nlines[q]; l += 112 * 256) accu += *(const unsigned*)(wp[q] + (size_t)l * 64);
    }
    if (accu == 0x9e3779b9u && tid == 999) gates[0] = 0.f;
  }
}

__global__ __launch_bounds__(256) void k_lnmod(const u16* __restrict__ x, const float* __restrict__ tab, int which,
                                               u16* __restrict__ out) {
  const int lane = threadIdx.x & 63, row = blockIdx.x * 4 + (threadIdx.x >> 6);
  const int b = row >> 10;
  const u16* xr = x + (size_t)row * DM;
  float v[12];
#pragma unroll
  for (int i = 0; i < 3; ++i) {
    u32x2 u = *(const u32x2*)(xr + i * 256 + lane * 4);
    v[i * 4 + 0] = bflo(u[0]); v[i * 4 + 1] = bfhi(u[0]);
    v[i * 4 + 2] = bflo(u[1]); v[i * 4 + 3] = bfhi(u[1]);
  }
  const float* S = tab + (size_t)(b * 2 + which) * 2 * DM;
  const float* Tt = S + DM;
  f32x4 sc[3], sh[3];
#pragma unroll
  for (int i = 0; i < 3; ++i) {
    sc[i] = *(const f32x4*)(S + i * 256 + lane * 4);
    sh[i] = *(const f32x4*)(Tt + i * 256 + lane * 4);
  }
  float s = 0.f;
#pragma unroll
  for (int i = 0; i < 12; ++i) s += v[i];
#pragma unroll
  for (int o = 32; o; o >>= 1) s += __shfl_xor(s, o);
  const float mean = s * (1.f / DM);
  float q = 0.f;
#pragma unroll
  for (int i = 0; i < 12; ++i) { float d = v[i] - mean; q += d * d; }
#pragma unroll
  for (int o = 32; o; o >>= 1) q += __shfl_xor(q, o);
  const float mb = rbf(mean), vb = rbf(q * (1.f / DM));
  const float sd = rbf(__fsqrt_rn(rbf(vb + 1e-5f)));
#pragma unroll
  for (int i = 0; i < 3; ++i) {
    float h[4];
#pragma unroll
    for (int j = 0; j < 4; ++j) {
      float n = rbf(rbf(v[i * 4 + j] - mb) / sd);
      h[j] = rbf(n * sc[i][j]) + sh[i][j];
    }
    u32x2 o = {pack2(h[0], h[1]), pack2(h[2], h[3])};
    *(u32x2*)(out + (size_t)row * DM + i * 256 + lane * 4) = o;
  }
}

using GQKV = GC<128, 144, 4, 2, 4, true, false>;
__global__ __launch_bounds__(512, 2) void k_qkv(const u16* __restrict__ h, const u16* __restrict__ w,
                                                u16* __restrict__ qkv) {
  extern __shared__ __attribute__((aligned(16))) char smem[];
  using C = GQKV;
  constexpr int BM = 128, BN = 144;
  const int xc = blockIdx.x & 7, j = blockIdx.x >> 3;
  const int tm = (xc >> 2) * 8 + (j >> 2), tn = (xc & 3) * 4 + (j & 3);
  const int m0 = tm * BM, n0 = tn * BN;
  f32x4 acc[C::MT][C::NT] = {};
  gemm_main<BM, BN, 4, 2, 4, true, 12, 0>(h + (size_t)m0 * DM, DM, w + n0, 3 * DM, smem, acc, NoHook(), NoHook());
  const int lane = threadIdx.x & 63, wave = threadIdx.x >> 6, wm = wave % 4, wn = wave / 4;
#pragma unroll
  for (int m = 0; m < C::MT; ++m) {
    const int row = m0 + wm * C::WTM + m * 16 + (lane & 15);
    u16* rp = qkv + (size_t)row * (3 * DM) + n0 + wn * C::WTN;
    u32x2 v[C::NT];
#pragma unroll
    for (int n = 0; n < C::NT; ++n)
      v[n] = u32x2{pack2(acc[m][n][0], acc[m][n][1]), pack2(acc[m][n][2], acc[m][n][3])};
    store_pair(rp, v[0], v[1]);
    store_pair(rp + 32, v[2], v[3]);
    if (wn == 0) *(u32x2*)(rp + 64 + (lane >> 4) * 4) = v[4];
  }
  STAMP(0, 3);
}

using GSC = GC<128, 128, 4, 2, 4, false, false>;
__global__ __launch_bounds__(512, 2) void k_scores(const u16* __restrict__ qkv, u16* __restrict__ E,
                                                   f32x2* __restrict__ st) {
  extern __shared__ __attribute__((aligned(16))) char smem[];
  using C = GSC;
  constexpr int BM = 128, BN = 128;
  const int xc = blockIdx.x & 7, j = blockIdx.x >> 3;
  const int bh = xc >> 1, b = bh >> 1, hh = bh & 1;
  const int tm = (xc & 1) * 4 + (j >> 3), tn = j & 7;
  const int m0 = tm * BM, n0 = tn * BN;
  const u16* Q = qkv + (size_t)(b * NSEQ + m0) * (3 * DM) + hh * DH;
  const u16* K = qkv + (size_t)(b * NSEQ + n0) * (3 * DM) + DM + hh * DH;
  f32x4 acc[C::MT][C::NT] = {};
  gemm_main<BM, BN, 4, 2, 4, false, 6, 1>(Q, 3 * DM, K, 3 * DM, smem, acc, NoHook(), NoHook());
  const int lane = threadIdx.x & 63, wave = threadIdx.x >> 6, wm = wave % 4, wn = wave / 4;
  const float scale = 0.05103103630798288f;
#pragma unroll
  for (int m = 0; m < C::MT; ++m) {
    float s[C::NT][4];
    float mx = -INFINITY;
#pragma unroll
    for (int n = 0; n < C::NT; ++n) {
      unsigned p0 = pack2(acc[m][n][0], acc[m][n][1]), p1 = pack2(acc[m][n][2], acc[m][n][3]);
      p0 = pack2(bflo(p0) * scale, bfhi(p0) * scale);
      p1 = pack2(bflo(p1) * scale, bfhi(p1) * scale);
      s[n][0] = bflo(p0); s[n][1] = bfhi(p0); s[n][2] = bflo(p1); s[n][3] = bfhi(p1);
      mx = fmaxf(mx, fmaxf(fmaxf(s[n][0], s[n][1]), fmaxf(s[n][2], s[n][3])));
    }
    mx = fmaxf(mx, __shfl_xor(mx, 16));
    mx = fmaxf(mx, __shfl_xor(mx, 32));
    float sum = 0.f;
    const int row = m0 + wm * C::WTM + m * 16 + (lane & 15);
    u16* Er = E + ((size_t)bh * NSEQ + row) * NSEQ + n0 + wn * C::WTN;
    u32x2 v[C::NT];
#pragma unroll
    for (int n = 0; n < C::NT; ++n) {
#pragma unroll
      for (int e = 0; e < 4; ++e) {
        s[n][e] = __expf(s[n][e] - mx);
        sum += s[n][e];
      }
      v[n] = u32x2{pack2(s[n][0], s[n][1]), pack2(s[n][2], s[n][3])};
    }
    store_pair(Er, v[0], v[1]);
    store_pair(Er + 32, v[2], v[3]);
    sum += __shfl_xor(sum, 16);
    sum += __shfl_xor(sum, 32);
    if (lane < 16) st[((size_t)bh * NSEQ + row) * 16 + tn * 2 + wn] = f32x2{mx, sum};
  }
  STAMP(1, 3);
}

using GPV = GC<64, 96, 4, 2, 6, true, false>;
__global__ __launch_bounds__(512, 2) void k_pv(const u16* __restrict__ E, const f32x2* __restrict__ st,
                                               const u16* __restrict__ qkv, u16* __restrict__ O) {
  extern __shared__ __attribute__((aligned(16))) char smem[];
  using C = GPV;
  constexpr int BM = 64, BN = 96;
  const int xc = blockIdx.x & 7, j = blockIdx.x >> 3;
  const int bh = xc >> 1, b = bh >> 1, hh = bh & 1;
  const int tm = (xc & 1) * 8 + (j >> 2), tn = j & 3;
  const int m0 = tm * BM, n0 = tn * BN;
  const int lane = threadIdx.x & 63, wave = threadIdx.x >> 6, wm = wave % 4, wn = wave / 4;
  float* ftab = (float*)(smem + C::LDS);
  auto pre = [&]() {
    if (threadIdx.x < 64) {
      const f32x4* p = (const f32x4*)(st + ((size_t)bh * NSEQ + m0 + threadIdx.x) * 16);
      f32x4 v[8];
#pragma unroll
      for (int t = 0; t < 8; ++t) v[t] = p[t];
      float M = -INFINITY;
#pragma unroll
      for (int t = 0; t < 8; ++t) M = fmaxf(M, fmaxf(v[t][0], v[t][2]));
      float fac[16], Ls = 0.f;
#pragma unroll
      for (int t = 0; t < 8; ++t) {
        fac[2 * t] = __expf(v[t][0] - M);
        fac[2 * t + 1] = __expf(v[t][2] - M);
        Ls += fac[2 * t] * v[t][1] + fac[2 * t + 1] * v[t][3];
      }
      float inv = 1.f / Ls;
#pragma unroll
      for (int t = 0; t < 16; ++t) ftab[t * 64 + threadIdx.x] = fac[t] * inv;
      asm volatile("s_waitcnt lgkmcnt(0)" ::: "memory");
    }
  };
  const u16* A = E + ((size_t)bh * NSEQ + m0) * NSEQ;
  const u16* V = qkv + (size_t)(b * NSEQ) * (3 * DM) + 2 * DM + hh * DH + n0;
  f32x4 acc[1][3] = {};
  f32x4 tot[1][3] = {};
  const float* frow = ftab + wm * 16 + (lane & 15);
  auto hook = [&](int t) {
    const float f = frow[t * 64];
#pragma unroll
    for (int n = 0; n < 3; ++n) {
      tot[0][n] += acc[0][n] * f;
      acc[0][n] = f32x4{0.f, 0.f, 0.f, 0.f};
    }
  };
  gemm_main<BM, BN, 4, 2, 6, true, 16, 2>(A, NSEQ, V, 3 * DM, smem, acc, pre, hook);
#pragma unroll
  for (int n = 0; n < 3; ++n) {
    int row = b * NSEQ + m0 + wm * 16 + (lane & 15);
    int col = hh * DH + n0 + wn * C::WTN + n * 16 + (lane >> 4) * 4;
    u32x2 v = {pack2(tot[0][n][0], tot[0][n][1]), pack2(tot[0][n][2], tot[0][n][3])};
    *(u32x2*)(O + (size_t)row * DM + col) = v;
  }
  STAMP(2, 3);
}

template <int KI, int LDA, bool STATS>
__device__ __forceinline__ void res_gemm(const u16* __restrict__ A, const u16* __restrict__ W,
                                         const u16* __restrict__ xin, const float* __restrict__ gates,
                                         u16* __restrict__ xo, f32x2* __restrict__ pst, char* smem) {
  using C = GPV;
  constexpr int BM = 64, BN = 96;
  const int xc = blockIdx.x & 7, j = blockIdx.x >> 3;
  const int tm = (xc >> 1) * 8 + (j >> 2), tn = (xc & 1) * 4 + (j & 3);
  const int m0 = tm * BM, n0 = tn * BN, b = m0 >> 10;
  const int lane = threadIdx.x & 63, wave = threadIdx.x >> 6, wm = wave % 4, wn = wave / 4;
  const int row = m0 + wm * 16 + (lane & 15);
  const int col0 = n0 + wn * C::WTN + (lane >> 4) * 4;
  f32x4 acc[1][3] = {};
  gemm_main<BM, BN, 4, 2, 6, true, LDA / 64, KI>(A + (size_t)m0 * LDA, LDA, W + n0, DM, smem, acc, NoHook(), NoHook());
  float vals[12];
#pragma unroll
  for (int n = 0; n < 3; ++n) {
    u32x2 xv = *(const u32x2*)(xin + (size_t)row * DM + col0 + n * 16);
    f32x4 gt = *(const f32x4*)(gates + b * (2 * DM) + col0 + n * 16);
    unsigned a0 = pack2(acc[0][n][0], acc[0][n][1]), a1 = pack2(acc[0][n][2], acc[0][n][3]);
    unsigned g0 = pack2(gt[0] * bflo(a0), gt[1] * bfhi(a0));
    unsigned g1 = pack2(gt[2] * bflo(a1), gt[3] * bfhi(a1));
    u32x2 o = {pack2(bflo(xv[0]) + bflo(g0), bfhi(xv[0]) + bfhi(g0)),
               pack2(bflo(xv[1]) + bflo(g1), bfhi(xv[1]) + bfhi(g1))};
    *(u32x2*)(xo + (size_t)row * DM + col0 + n * 16) = o;
    vals[n * 4 + 0] = bflo(o[0]); vals[n * 4 + 1] = bfhi(o[0]);
    vals[n * 4 + 2] = bflo(o[1]); vals[n * 4 + 3] = bfhi(o[1]);
  }
  if (STATS) {
    float s = 0.f;
#pragma unroll
    for (int i = 0; i < 12; ++i) s += vals[i];
    s += __shfl_xor(s, 16);
    s += __shfl_xor(s, 32);
    const float lm = s * (1.f / 48.f);
    float q = 0.f;
#pragma unroll
    for (int i = 0; i < 12; ++i) { float d = vals[i] - lm; q += d * d; }
    q += __shfl_xor(q, 16);
    q += __shfl_xor(q, 32);
    if (lane < 16) pst[(size_t)row * 16 + tn * 2 + wn] = f32x2{s, q};
  }
  STAMP(KI, 3);
}
__global__ __launch_bounds__(512, 2) void k_wo(const u16* __restrict__ O, const u16* __restrict__ wo,
                                               const u16* __restrict__ xin, const float* __restrict__ gates,
                                               u16* __restrict__ x1, f32x2* __restrict__ pst) {
  extern __shared__ __attribute__((aligned(16))) char smem[];
  res_gemm<3, DM, false>(O, wo, xin, gates, x1, pst, smem);
}
__global__ __launch_bounds__(512, 2) void k_fc2(const u16* __restrict__ f, const u16* __restrict__ w2,
                                                const u16* __restrict__ x1, const float* __restrict__ gates,
                                                u16* __restrict__ out) {
  extern __shared__ __attribute__((aligned(16))) char smem[];
  res_gemm<5, FF, false>(f, w2, x1, gates + DM, out, nullptr, smem);
}

__device__ __forceinline__ float gelu_tanh(float u) {
  float w = u * (-2.f * 0.7978845608028654f * 1.4426950408889634f);
  float a = w * (1.f + 0.044715f * u * u);
  return u * __builtin_amdgcn_rcpf(1.f + __builtin_amdgcn_exp2f(a));
}
using GFC = GC<128, 192, 2, 4, 4, true, false>;
__global__ __launch_bounds__(512, 2) void k_fc1(const u16* __restrict__ h2, const u16* __restrict__ w1,
                                                u16* __restrict__ f) {
  extern __shared__ __attribute__((aligned(16))) char smem[];
  using C = GFC;
  constexpr int BM = 128, BN = 192;
  const int xc = blockIdx.x & 7, j = blockIdx.x >> 3;
  const int tm = (xc >> 2) * 8 + (j >> 2), tn = (xc & 3) * 4 + (j & 3);
  const int m0 = tm * BM, n0 = tn * BN;
  f32x4 acc[C::MT][C::NT] = {};
  gemm_main<BM, BN, 2, 4, 4, true, 12, 4>(h2 + (size_t)m0 * DM, DM, w1 + n0, FF, smem, acc, NoHook(), NoHook());
  const int lane = threadIdx.x & 63, wave = threadIdx.x >> 6, wm = wave % 2, wn = wave / 2;
#pragma unroll
  for (int m = 0; m < C::MT; ++m) {
    const int row = m0 + wm * C::WTM + m * 16 + (lane & 15);
    u16* rp = f + (size_t)row * FF + n0 + wn * C::WTN;
    u32x2 v[C::NT];
#pragma unroll
    for (int n = 0; n < C::NT; ++n) {
      unsigned p0 = pack2(acc[m][n][0], acc[m][n][1]), p1 = pack2(acc[m][n][2], acc[m][n][3]);
      v[n] = u32x2{pack2(gelu_tanh(bflo(p0)), gelu_tanh(bfhi(p0))), pack2(gelu_tanh(bflo(p1)), gelu_tanh(bfhi(p1)))};
    }
    store_pair(rp, v[0], v[1]);
    *(u32x2*)(rp + 32 + (lane >> 4) * 4) = v[2];
  }
  STAMP(4, 3);
}

extern "C" void kernel_launch(void* const* d_in, const int* in_sizes, int n_in, void* d_out, int out_size,
                              void* d_ws, size_t ws_size, hipStream_t stream) {
  const u16* x = (const u16*)d_in[0];
  const u16* c = (const u16*)d_in[1];
  const u16* wqkv = (const u16*)d_in[2];
  const u16* wo = (const u16*)d_in[3];
  const u16* wmod = (const u16*)d_in[4];
  const u16* w1 = (const u16*)d_in[5];
  const u16* w2 = (const u16*)d_in[6];
  u16* out = (u16*)d_out;

  char* ws = (char*)d_ws;
  size_t o = 0;
  auto take = [&](size_t bytes) { char* p = ws + o; o += (bytes + 255) & ~(size_t)255; return p; };
  float* gates = (float*)take(2 * 2 * DM * 4);
  float* tab = (float*)take(2 * 2 * 2 * DM * 4);
  u16* h = (u16*)take((size_t)MROWS * DM * 2);
  u16* h2 = (u16*)take((size_t)MROWS * DM * 2);
  u16* qkv = (u16*)take((size_t)MROWS * 3 * DM * 2);
  u16* E = (u16*)take((size_t)4 * NSEQ * NSEQ * 2);
  f32x2* st = (f32x2*)take((size_t)4 * NSEQ * 16 * 8);
  u16* O = (u16*)take((size_t)MROWS * DM * 2);
  u16* x1 = (u16*)take((size_t)MROWS * DM * 2);
  f32x2* pst = (f32x2*)take((size_t)MROWS * 16 * 8);
  u16* f = (u16*)take((size_t)MROWS * FF * 2);

  constexpr int LDS_QKV = GQKV::LDS;
  constexpr int LDS_SC = GSC::LDS;
  constexpr int LDS_PV = GPV::LDS + 16 * 64 * 4;
  constexpr int LDS_WO = GPV::LDS;
  constexpr int LDS_FC = GFC::LDS;
  (void)hipFuncSetAttribute((const void*)k_qkv, hipFuncAttributeMaxDynamicSharedMemorySize, LDS_QKV);
  (void)hipFuncSetAttribute((const void*)k_scores, hipFuncAttributeMaxDynamicSharedMemorySize, LDS_SC);
  (void)hipFuncSetAttribute((const void*)k_pv, hipFuncAttributeMaxDynamicSharedMemorySize, LDS_PV);
  (void)hipFuncSetAttribute((const void*)k_wo, hipFuncAttributeMaxDynamicSharedMemorySize, LDS_WO);
  (void)hipFuncSetAttribute((const void*)k_fc1, hipFuncAttributeMaxDynamicSharedMemorySize, LDS_FC);
  (void)hipFuncSetAttribute((const void*)k_fc2, hipFuncAttributeMaxDynamicSharedMemorySize, LDS_WO);

  k_pre<<<144 + 112, 256, 0, stream>>>(c, wmod, gates, tab, wqkv, wo, w1, w2);
  k_lnmod<<<MROWS / 4, 256, 0, stream>>>(x, tab, 0, h);
  k_qkv<<<256, 512, LDS_QKV, stream>>>(h, wqkv, qkv);
  k_scores<<<256, 512, LDS_SC, stream>>>(qkv, E, st);
  k_pv<<<256, 512, LDS_PV, stream>>>(E, st, qkv, O);
  k_wo<<<256, 512, LDS_WO, stream>>>(O, wo, x, gates, x1, pst);
  k_lnmod<<<MROWS / 4, 256, 0, stream>>>(x1, tab, 1, h2);
  k_fc1<<<256, 512, LDS_FC, stream>>>(h2, w1, f);
  k_fc2<<<256, 512, LDS_WO, stream>>>(f, w2, x1, gates, out);
}
```

```cpp
#include <hip/hip_runtime.h>
#include <hip/hip_bf16.h>
#include <stdint.h>

typedef unsigned short u16;
typedef __attribute__((ext_vector_type(8))) __bf16 bf16x8;
typedef __attribute__((ext_vector_type(4))) __bf16 bf16x4;
typedef __attribute__((ext_vector_type(2))) __bf16 bf16x2;
typedef __attribute__((ext_vector_type(4))) float f32x4;
typedef __attribute__((ext_vector_type(2))) float f32x2;
typedef __attribute__((ext_vector_type(4))) unsigned u32x4;
typedef __attribute__((ext_vector_type(2))) unsigned u32x2;
#define LAS __attribute__((address_space(3)))
#define GAS __attribute__((address_space(1)))

constexpr int NB = 2, NSEQ = 1024, DM = 768, NH = 2, DH = 384, FF = 3072;
constexpr int MROWS = NB * NSEQ;

__device__ __forceinline__ float bf2f(u16 v) { return __uint_as_float(((unsigned)v) << 16); }
__device__ __forceinline__ float bflo(unsigned v) { return __uint_as_float(v << 16); }
__device__ __forceinline__ float bfhi(unsigned v) { return __uint_as_float(v & 0xffff0000u); }
__device__ __forceinline__ unsigned pack2(float a, float b) {
  f32x2 f = {a, b};
  bf16x2 r = __builtin_convertvector(f, bf16x2);
  return __builtin_bit_cast(unsigned, r);
}
__device__ __forceinline__ float rbf(float a) {
  return bflo(pack2(a, 0.f) & 0xffffu);
}

template <int N>
__device__ __forceinline__ void waitv() {
  asm volatile("s_waitcnt vmcnt(%0)" ::"i"(N) : "memory");
}
template <int I>
struct IC {
  static constexpr int value = I;
};
template <int B, int E, class F>
__device__ __forceinline__ void static_for(F&& f) {
  if constexpr (B < E) {
    f(IC<B>{});
    static_for<B + 1, E>(f);
  }
}
#define ABL 0
#define FENCE() asm volatile("" ::: "memory")
#define STAMP(kid, i)
#define BARRIER()                        \
  do {                                   \
    FENCE();                             \
    __builtin_amdgcn_s_barrier();        \
    FENCE();                             \
  } while (0)

template <int BM, int BN, int WM, int WN, int NS, bool BKN, bool ATR>
struct GC {
  static constexpr int NW = WM * WN;
  static constexpr int WTM = BM / WM, MT = WTM / 16;
  static constexpr int NTT = BN / 16;
  static constexpr int NT = (NTT + WN - 1) / WN;
  static constexpr int WTN = NT * 16;
  static constexpr int A_BYTES = BM * 128, B_BYTES = BN * 128;
  static constexpr int NSA = ATR ? 3 : NS;
  static constexpr int B_OFF = NSA * A_BYTES;
  static constexpr int TAB_OFF = B_OFF + NS * B_BYTES;
  static constexpr int NA = BM / 8, NBI = BN / 8;
  static constexpr int LA = ATR ? 0 : NA / 4;
  static constexpr int LB = (NBI + 3) / 4;
  static constexpr int NFULL = NBI % 4 == 0 ? 4 : NBI % 4;
  static constexpr int L = LA + LB;
  static constexpr int LDS = TAB_OFF + (ATR ? 8192 : 0);
  static_assert(NW == 8, "8 waves");
  static_assert(NA % 4 == 0, "A instr split");
  static_assert(NS >= 4, "stages");
  static_assert(LDS <= 163840, "lds");
};

template <class C, int BN, int WM, int WN, bool BKN>
struct GemmCtx {
  static constexpr int MT = C::MT, NT = C::NT, L = C::L, LA = C::LA, LB = C::LB;
  const char* Abase;
  const char* Bbase;
  size_t bstep;
  char* smem;
  unsigned goff[L > 0 ? L : 1];
  unsigned aoff[2], boff[2];
  int wq, par, last_n;
  bool full;

  __device__ __forceinline__ void init(const u16* Ab, int lda, const u16* Bb, int ldb, char* sm) {
    const int tid = threadIdx.x, lane = tid & 63;
    const int wave = __builtin_amdgcn_readfirstlane(tid >> 6);
    const int wm = wave % WM, wn = wave / WM;
    wq = wave & 3;
    par = wave >> 2;
    full = (C::NFULL == 4) || (wq < C::NFULL);
    smem = sm;
#pragma unroll
    for (int i = 0; i < LA; ++i) {
      int r = i * 4 + wq;
      int row = r * 8 + (lane >> 3);
      int lc = (lane & 7) ^ (row & 7);
      goff[i] = (unsigned)(row * lda + lc * 8) * 2u;
    }
#pragma unroll
    for (int i = 0; i < LB; ++i) {
      int r = i * 4 + wq;
      if (r >= C::NBI) r = 0;
      if (BKN) {
        int st = r * 4 + (lane >> 4);
        int kb = st / (BN / 16), nb = st % (BN / 16);
        int i16 = lane & 15, prow = i16 >> 1, hf = i16 & 1;
        int lrow = prow ^ ((kb & 1) << 2);
        goff[LA + i] = (unsigned)((kb * 8 + lrow) * ldb + nb * 16 + hf * 8) * 2u;
      } else {
        int row = r * 8 + (lane >> 3);
        int lc = (lane & 7) ^ (row & 7);
        goff[LA + i] = (unsigned)(row * ldb + lc * 8) * 2u;
      }
    }
    Abase = (const char*)Ab;
    Bbase = (const char*)Bb;
    bstep = BKN ? (size_t)64 * ldb * 2 : (size_t)128;
    const int g = lane >> 4, i16 = lane & 15;
#pragma unroll
    for (int s = 0; s < 2; ++s)
      aoff[s] = (unsigned)((wm * C::WTM + i16) * 128 + (((s * 4 + g) ^ (lane & 7)) << 4));
    if (BKN) {
      const int q4 = i16 >> 2, p = i16 & 3;
#pragma unroll
      for (int h = 0; h < 2; ++h)
        boff[h] = (unsigned)(C::B_OFF + (g * (BN / 16) + wn * NT) * 256 + (((h * 4 + q4) ^ ((g & 1) << 2)) * 32) +
                             p * 8);
    } else {
#pragma unroll
      for (int s = 0; s < 2; ++s)
        boff[s] = (unsigned)(C::B_OFF + (wn * C::WTN + i16) * 128 + (((s * 4 + g) ^ (lane & 7)) << 4));
    }
    const bool last_dummy = (C::NTT % WN != 0) && (wn * NT + NT > C::NTT);
    last_n = last_dummy ? 0 : NT - 1;
  }
  template <int i>
  __device__ __forceinline__ void issue1(int t, int sl) {
    if constexpr (i < LA) {
      __builtin_amdgcn_global_load_lds((const GAS unsigned*)(Abase + (size_t)t * 128 + goff[i]),
                                       (LAS unsigned*)(smem + sl * C::A_BYTES + wq * 1024 + i * 4096), 16, 0, 0);
    } else {
      if (i + 1 < L || full)
        __builtin_amdgcn_global_load_lds(
            (const GAS unsigned*)(Bbase + (size_t)t * bstep + goff[i]),
            (LAS unsigned*)(smem + C::B_OFF + sl * C::B_BYTES + wq * 1024 + (i - LA) * 4096), 16, 0, 0);
    }
  }
  __device__ __forceinline__ void issue_all(int t, int sl) {
    static_for<0, L>([&](auto II) { issue1<II.value>(t, sl); });
  }
  template <int n, int x>
  __device__ __forceinline__ void wait_st() {
    if (C::NFULL == 4 || n == 0) {
      waitv<n * L + x>();
    } else {
      if (full) waitv<n * L + x>();
      else waitv<n * (L - 1) + x>();
    }
  }
  template <int s>
  __device__ __forceinline__ void rd_frag(int sla, int slb, bf16x8 (&fa)[MT], bf16x8 (&fb)[NT]) {
    const char* sa = smem + sla * C::A_BYTES;
    const char* sb = smem + slb * C::B_BYTES;
#pragma unroll
    for (int m = 0; m < MT; ++m) fa[m] = *(const bf16x8*)(sa + aoff[s] + m * 2048);
#pragma unroll
    for (int n = 0; n < NT; ++n) {
      const int nn = (n == NT - 1) ? last_n : n;
      if (BKN) {
        bf16x4 lo = __builtin_amdgcn_ds_read_tr16_b64_v4bf16(
            (LAS bf16x4*)(sb + boff[0] + s * 4 * (BN / 16) * 256 + nn * 256));
        bf16x4 hi = __builtin_amdgcn_ds_read_tr16_b64_v4bf16(
            (LAS bf16x4*)(sb + boff[1] + s * 4 * (BN / 16) * 256 + nn * 256));
        fb[n] = __builtin_shufflevector(lo, hi, 0, 1, 2, 3, 4, 5, 6, 7);
      } else {
        fb[n] = *(const bf16x8*)(sb + boff[s] + nn * 2048);
      }
    }
  }
  template <bool ISS>
  __device__ __forceinline__ void mma(f32x4 (&acc)[MT][NT], bf16x8 (&fa)[MT], bf16x8 (&fb)[NT], int ti, int sl,
                                      bool do_issue) {
    static_for<0, MT * NT>([&](auto Q) {
      constexpr int q = Q.value, m = q / NT, n = q % NT;
      acc[m][n] = __builtin_amdgcn_mfma_f32_16x16x32_bf16(fb[n], fa[m], acc[m][n], 0, 0, 0);
      if constexpr (ISS) {
        constexpr int lo = (q * L) / (MT * NT), hi = ((q + 1) * L) / (MT * NT);
        if constexpr (hi > lo) {
          if (do_issue) static_for<lo, hi>([&](auto II) { issue1<II.value>(ti, sl); });
          __builtin_amdgcn_sched_barrier(0);
        }
      }
    });
  }
};

template <int BM, int BN, int WM, int WN, int NS, bool BKN, int T, int KID, class Pre, class Hook>
__device__ __forceinline__ void gemm_main(const u16* __restrict__ Ab, int lda, const u16* __restrict__ Bb, int ldb,
                                          char* smem,
                                          f32x4 (&acc)[GC<BM, BN, WM, WN, NS, BKN, false>::MT][GC<BM, BN, WM, WN, NS, BKN, false>::NT],
                                          Pre pre, Hook hook) {
  using C = GC<BM, BN, WM, WN, NS, BKN, false>;
  constexpr int MT = C::MT, NT = C::NT;
  GemmCtx<C, BN, WM, WN, BKN> g;
  g.init(Ab, lda, Bb, ldb, smem);
  STAMP(KID, 0);
  static_for<0, NS - 1>([&](auto S) {
    if ((S.value & 1) == g.par) g.issue_all(S.value, S.value);
  });
  pre();
  if (g.par == 0) g.template wait_st<(NS - 2) / 2, 0>();
  BARRIER();
  STAMP(KID, 1);
  bf16x8 fa0[MT], fb0[NT], fa1[MT], fb1[NT];
  g.template rd_frag<0>(0, 0, fa0, fb0);
  constexpr int U = (NS % 2 == 0) ? NS : 2 * NS;
  constexpr int NSTEADY = T - NS + 1;
  constexpr int NBLK = NSTEADY / U;
#pragma unroll 1
  for (int tb = 0; tb < NBLK * U; tb += U) {
    static_for<0, U>([&](auto UU) {
      constexpr int u = UU.value;
      const int t = tb + u;
      g.template rd_frag<1>(u % NS, u % NS, fa1, fb1);
      __builtin_amdgcn_sched_barrier(0);
      g.template mma<false>(acc, fa0, fb0, 0, 0, false);
      __builtin_amdgcn_sched_barrier(0);
      if (g.par == ((u + 1) & 1)) g.template wait_st<(NS - 3) / 2, 0>();
      BARRIER();
      g.template rd_frag<0>((u + 1) % NS, (u + 1) % NS, fa0, fb0);
      __builtin_amdgcn_sched_barrier(0);
      g.template mma<true>(acc, fa1, fb1, t + NS - 1, (u + NS - 1) % NS, g.par == ((u + NS - 1) & 1));
      hook(t);
      __builtin_amdgcn_sched_barrier(0);
    });
  }
  static_for<NBLK * U, NSTEADY>([&](auto TT) {
    constexpr int t = TT.value;
    g.template rd_frag<1>(t % NS, t % NS, fa1, fb1);
    __builtin_amdgcn_sched_barrier(0);
    g.template mma<false>(acc, fa0, fb0, 0, 0, false);
    __builtin_amdgcn_sched_barrier(0);
    if (g.par == ((t + 1) & 1)) g.template wait_st<(NS - 3) / 2, 0>();
    BARRIER();
    g.template rd_frag<0>((t + 1) % NS, (t + 1) % NS, fa0, fb0);
    __builtin_amdgcn_sched_barrier(0);
    g.template mma<true>(acc, fa1, fb1, t + NS - 1, (t + NS - 1) % NS, g.par == ((t + NS - 1) & 1));
    hook(t);
    __builtin_amdgcn_sched_barrier(0);
  });
  static_for<T - NS + 1, T>([&](auto TT) {
    constexpr int t = TT.value;
    g.template rd_frag<1>(t % NS, t % NS, fa1, fb1);
    __builtin_amdgcn_sched_barrier(0);
    g.template mma<false>(acc, fa0, fb0, 0, 0, false);
    __builtin_amdgcn_sched_barrier(0);
    if constexpr (t + 1 < T) {
      constexpr int younger = (T - 1) >= t + 3 ? (T - 1 - (t + 1)) / 2 : 0;
      if (g.par == ((t + 1) & 1)) g.template wait_st<younger, 0>();
      BARRIER();
      g.template rd_frag<0>((t + 1) % NS, (t + 1) % NS, fa0, fb0);
      __builtin_amdgcn_sched_barrier(0);
    }
    g.template mma<false>(acc, fa1, fb1, 0, 0, false);
    hook(t);
    __builtin_amdgcn_sched_barrier(0);
  });
  STAMP(KID, 2);
}

__device__ __forceinline__ void store_pair(u16* rowp, u32x2 a, u32x2 b) {
  auto rx = __builtin_amdgcn_permlane16_swap(a[0], b[0], false, false);
  auto ry = __builtin_amdgcn_permlane16_swap(a[1], b[1], false, false);
  const int g = (threadIdx.x & 63) >> 4;
  u32x4 v = {rx[0], ry[0], rx[1], ry[1]};
  *(u32x4*)(rowp + (g & 1) * 16 + (g >> 1) * 8) = v;
}

struct NoHook {
  __device__ __forceinline__ void operator()(int) const {}
  __device__ __forceinline__ void operator()() const {}
};

__device__ __forceinline__ f32x2 ln_params(float mean, float var) {
  const float mb = rbf(mean), vb = rbf(var);
  const float sd = rbf(__fsqrt_rn(rbf(vb + 1e-5f)));
  const float ra = 1.f / sd;
  return f32x2{ra, -mb * ra};
}
__global__ __launch_bounds__(256) void k_pre(const u16* __restrict__ c, const u16* __restrict__ wmod,
                                             float* __restrict__ gates, float* __restrict__ tab,
                                             const u16* __restrict__ pw0, const u16* __restrict__ pw1,
                                             const u16* __restrict__ pw2, const u16* __restrict__ pw3,
                                             const unsigned* __restrict__ wsb, int ws_pages) {
  __shared__ float red[2][64][33];
  const int tid = threadIdx.x;
  if (blockIdx.x < 144) {
    const int cg = tid & 3, kq = tid >> 2;
    const int col0 = blockIdx.x * 32 + cg * 8;
    u32x4 w[12];
#pragma unroll
    for (int i = 0; i < 12; ++i) {
      int k = i * 64 + kq;
      w[i] = *(const u32x4*)(wmod + (size_t)k * (6 * DM) + col0);
    }
    float a0[8], a1[8];
#pragma unroll
    for (int j = 0; j < 8; ++j) a0[j] = a1[j] = 0.f;
#pragma unroll
    for (int i = 0; i < 12; ++i) {
      int k = i * 64 + kq;
      float c0 = bf2f(c[k]), c1 = bf2f(c[DM + k]);
      float s0 = rbf(c0 / (1.f + __expf(-c0)));
      float s1 = rbf(c1 / (1.f + __expf(-c1)));
#pragma unroll
      for (int j = 0; j < 4; ++j) {
        float wl = bflo(w[i][j]), wh = bfhi(w[i][j]);
        a0[2 * j] += s0 * wl; a0[2 * j + 1] += s0 * wh;
        a1[2 * j] += s1 * wl; a1[2 * j + 1] += s1 * wh;
      }
    }
#pragma unroll
    for (int j = 0; j < 8; ++j) {
      red[0][kq][cg * 8 + j] = a0[j];
      red[1][kq][cg * 8 + j] = a1[j];
    }
    __syncthreads();
    if (tid < 64) {
      int b = tid >> 5, cl = tid & 31;
      float s = 0.f;
      for (int q = 0; q < 64; ++q) s += red[b][q][cl];
      const float v = rbf(s);
      const int col = blockIdx.x * 32 + cl, i = col / DM, cc = col % DM;
      const int w_ = i / 3, kind = i % 3;
      if (kind == 2) gates[(b * 2 + w_) * DM + cc] = v;
      else if (kind == 1) tab[((b * 2 + w_) * 2 + 0) * DM + cc] = rbf(1.f + v);
      else tab[((b * 2 + w_) * 2 + 1) * DM + cc] = v;
    }
  } else {
    const int wb = blockIdx.x - 144;
    const u16* wp[4] = {pw0, pw1, pw2, pw3};
    const int nlines[4] = {DM * 3 * DM / 64, DM * DM / 64, DM * FF / 64, FF * DM / 64};
    unsigned accu = 0;
#pragma unroll
    for (int q = 0; q < 4; ++q) {
      for (int l = wb * 256 + tid; l < nlines[q]; l += 112 * 256) accu += *(const unsigned*)(wp[q] + (size_t)l * 64);
    }
    {
      const int part = wb >> 3;
      for (int pg = part * 256 + tid; pg < ws_pages; pg += 14 * 256) accu += wsb[(size_t)pg * 1024];
    }
    if (accu == 0x9e3779b9u && tid == 999) gates[0] = 0.f;
  }
}

__global__ __launch_bounds__(256) void k_lnmod(const u16* __restrict__ x, const float* __restrict__ tab, int which,
                                               u16* __restrict__ out) {
  const int lane = threadIdx.x & 63, row = blockIdx.x * 4 + (threadIdx.x >> 6);
  const int b = row >> 10;
  const u16* xr = x + (size_t)row * DM;
  float v[12];
#pragma unroll
  for (int i = 0; i < 3; ++i) {
    u32x2 u = *(const u32x2*)(xr + i * 256 + lane * 4);
    v[i * 4 + 0] = bflo(u[0]); v[i * 4 + 1] = bfhi(u[0]);
    v[i * 4 + 2] = bflo(u[1]); v[i * 4 + 3] = bfhi(u[1]);
  }
  const float* S = tab + (size_t)(b * 2 + which) * 2 * DM;
  const float* Tt = S + DM;
  f32x4 sc[3], sh[3];
#pragma unroll
  for (int i = 0; i < 3; ++i) {
    sc[i] = *(const f32x4*)(S + i * 256 + lane * 4);
    sh[i] = *(const f32x4*)(Tt + i * 256 + lane * 4);
  }
  float s = 0.f;
#pragma unroll
  for (int i = 0; i < 12; ++i) s += v[i];
#pragma unroll
  for (int o = 32; o; o >>= 1) s += __shfl_xor(s, o);
  const float mean = s * (1.f / DM);
  float q = 0.f;
#pragma unroll
  for (int i = 0; i < 12; ++i) { float d = v[i] - mean; q += d * d; }
#pragma unroll
  for (int o = 32; o; o >>= 1) q += __shfl_xor(q, o);
  const float mb = rbf(mean), vb = rbf(q * (1.f / DM));
  const float sd = rbf(__fsqrt_rn(rbf(vb + 1e-5f)));
#pragma unroll
  for (int i = 0; i < 3; ++i) {
    float h[4];
#pragma unroll
    for (int j = 0; j < 4; ++j) {
      float n = rbf(rbf(v[i * 4 + j] - mb) / sd);
      h[j] = rbf(n * sc[i][j]) + sh[i][j];
    }
    u32x2 o = {pack2(h[0], h[1]), pack2(h[2], h[3])};
    *(u32x2*)(out + (size_t)row * DM + i * 256 + lane * 4) = o;
  }
}

using GQKV = GC<128, 144, 4, 2, 4, true, false>;
__global__ __launch_bounds__(512, 2) void k_qkv(const u16* __restrict__ h, const u16* __restrict__ w,
                                                u16* __restrict__ qkv) {
  extern __shared__ __attribute__((aligned(16))) char smem[];
  using C = GQKV;
  constexpr int BM = 128, BN = 144;
  const int xc = blockIdx.x & 7, j = blockIdx.x >> 3;
  const int tm = (xc >> 2) * 8 + (j >> 2), tn = (xc & 3) * 4 + (j & 3);
  const int m0 = tm * BM, n0 = tn * BN;
  f32x4 acc[C::MT][C::NT] = {};
  gemm_main<BM, BN, 4, 2, 4, true, 12, 0>(h + (size_t)m0 * DM, DM, w + n0, 3 * DM, smem, acc, NoHook(), NoHook());
  const int lane = threadIdx.x & 63, wave = threadIdx.x >> 6, wm = wave % 4, wn = wave / 4;
#pragma unroll
  for (int m = 0; m < C::MT; ++m) {
    const int row = m0 + wm * C::WTM + m * 16 + (lane & 15);
    u16* rp = qkv + (size_t)row * (3 * DM) + n0 + wn * C::WTN;
    u32x2 v[C::NT];
#pragma unroll
    for (int n = 0; n < C::NT; ++n)
      v[n] = u32x2{pack2(acc[m][n][0], acc[m][n][1]), pack2(acc[m][n][2], acc[m][n][3])};
    store_pair(rp, v[0], v[1]);
    store_pair(rp + 32, v[2], v[3]);
    if (wn == 0) *(u32x2*)(rp + 64 + (lane >> 4) * 4) = v[4];
  }
  STAMP(0, 3);
}

using GSC = GC<128, 128, 4, 2, 4, false, false>;
__global__ __launch_bounds__(512, 2) void k_scores(const u16* __restrict__ qkv, u16* __restrict__ E,
                                                   f32x2* __restrict__ st) {
  extern __shared__ __attribute__((aligned(16))) char smem[];
  using C = GSC;
  constexpr int BM = 128, BN = 128;
  const int xc = blockIdx.x & 7, j = blockIdx.x >> 3;
  const int bh = xc >> 1, b = bh >> 1, hh = bh & 1;
  const int tm = (xc & 1) * 4 + (j >> 3), tn = j & 7;
  const int m0 = tm * BM, n0 = tn * BN;
  const u16* Q = qkv + (size_t)(b * NSEQ + m0) * (3 * DM) + hh * DH;
  const u16* K = qkv + (size_t)(b * NSEQ + n0) * (3 * DM) + DM + hh * DH;
  f32x4 acc[C::MT][C::NT] = {};
  gemm_main<BM, BN, 4, 2, 4, false, 6, 1>(Q, 3 * DM, K, 3 * DM, smem, acc, NoHook(), NoHook());
  const int lane = threadIdx.x & 63, wave = threadIdx.x >> 6, wm = wave % 4, wn = wave / 4;
  const float scale = 0.05103103630798288f;
#pragma unroll
  for (int m = 0; m < C::MT; ++m) {
    float s[C::NT][4];
    float mx = -INFINITY;
#pragma unroll
    for (int n = 0; n < C::NT; ++n) {
      unsigned p0 = pack2(acc[m][n][0], acc[m][n][1]), p1 = pack2(acc[m][n][2], acc[m][n][3]);
      p0 = pack2(bflo(p0) * scale, bfhi(p0) * scale);
      p1 = pack2(bflo(p1) * scale, bfhi(p1) * scale);
      s[n][0] = bflo(p0); s[n][1] = bfhi(p0); s[n][2] = bflo(p1); s[n][3] = bfhi(p1);
      mx = fmaxf(mx, fmaxf(fmaxf(s[n][0], s[n][1]), fmaxf(s[n][2], s[n][3])));
    }
    mx = fmaxf(mx, __shfl_xor(mx, 16));
    mx = fmaxf(mx, __shfl_xor(mx, 32));
    float sum = 0.f;
    const int row = m0 + wm * C::WTM + m * 16 + (lane & 15);
    u16* Er = E + ((size_t)bh * NSEQ + row) * NSEQ + n0 + wn * C::WTN;
    u32x2 v[C::NT];
#pragma unroll
    for (int n = 0; n < C::NT; ++n) {
#pragma unroll
      for (int e = 0; e < 4; ++e) {
        s[n][e] = __expf(s[n][e] - mx);
        sum += s[n][e];
      }
      v[n] = u32x2{pack2(s[n][0], s[n][1]), pack2(s[n][2], s[n][3])};
    }
    store_pair(Er, v[0], v[1]);
    store_pair(Er + 32, v[2], v[3]);
    sum += __shfl_xor(sum, 16);
    sum += __shfl_xor(sum, 32);
    if (lane < 16) st[((size_t)bh * NSEQ + row) * 16 + tn * 2 + wn] = f32x2{mx, sum};
  }
  STAMP(1, 3);
}

using GPV = GC<64, 96, 4, 2, 6, true, false>;
__global__ __launch_bounds__(512, 2) void k_pv(const u16* __restrict__ E, const f32x2* __restrict__ st,
                                               const u16* __restrict__ qkv, u16* __restrict__ O) {
  extern __shared__ __attribute__((aligned(16))) char smem[];
  using C = GPV;
  constexpr int BM = 64, BN = 96;
  const int xc = blockIdx.x & 7, j = blockIdx.x >> 3;
  const int bh = xc >> 1, b = bh >> 1, hh = bh & 1;
  const int tm = (xc & 1) * 8 + (j >> 2), tn = j & 3;
  const int m0 = tm * BM, n0 = tn * BN;
  const int lane = threadIdx.x & 63, wave = threadIdx.x >> 6, wm = wave % 4, wn = wave / 4;
  float* ftab = (float*)(smem + C::LDS);
  auto pre = [&]() {
    if (threadIdx.x < 64) {
      const f32x4* p = (const f32x4*)(st + ((size_t)bh * NSEQ + m0 + threadIdx.x) * 16);
      f32x4 v[8];
#pragma unroll
      for (int t = 0; t < 8; ++t) v[t] = p[t];
      float M = -INFINITY;
#pragma unroll
      for (int t = 0; t < 8; ++t) M = fmaxf(M, fmaxf(v[t][0], v[t][2]));
      float fac[16], Ls = 0.f;
#pragma unroll
      for (int t = 0; t < 8; ++t) {
        fac[2 * t] = __expf(v[t][0] - M);
        fac[2 * t + 1] = __expf(v[t][2] - M);
        Ls += fac[2 * t] * v[t][1] + fac[2 * t + 1] * v[t][3];
      }
      float inv = 1.f / Ls;
#pragma unroll
      for (int t = 0; t < 16; ++t) ftab[t * 64 + threadIdx.x] = fac[t] * inv;
      asm volatile("s_waitcnt lgkmcnt(0)" ::: "memory");
    }
  };
  const u16* A = E + ((size_t)bh * NSEQ + m0) * NSEQ;
  const u16* V = qkv + (size_t)(b * NSEQ) * (3 * DM) + 2 * DM + hh * DH + n0;
  f32x4 acc[1][3] = {};
  f32x4 tot[1][3] = {};
  const float* frow = ftab + wm * 16 + (lane & 15);
  auto hook = [&](int t) {
    const float f = frow[t * 64];
#pragma unroll
    for (int n = 0; n < 3; ++n) {
      tot[0][n] += acc[0][n] * f;
      acc[0][n] = f32x4{0.f, 0.f, 0.f, 0.f};
    }
  };
  gemm_main<BM, BN, 4, 2, 6, true, 16, 2>(A, NSEQ, V, 3 * DM, smem, acc, pre, hook);
#pragma unroll
  for (int n = 0; n < 3; ++n) {
    int row = b * NSEQ + m0 + wm * 16 + (lane & 15);
    int col = hh * DH + n0 + wn * C::WTN + n * 16 + (lane >> 4) * 4;
    u32x2 v = {pack2(tot[0][n][0], tot[0][n][1]), pack2(tot[0][n][2], tot[0][n][3])};
    *(u32x2*)(O + (size_t)row * DM + col) = v;
  }
  STAMP(2, 3);
}

template <int KI, int LDA, bool STATS>
__device__ __forceinline__ void res_gemm(const u16* __restrict__ A, const u16* __restrict__ W,
                                         const u16* __restrict__ xin, const float* __restrict__ gates,
                                         u16* __restrict__ xo, f32x2* __restrict__ pst, char* smem) {
  using C = GPV;
  constexpr int BM = 64, BN = 96;
  const int xc = blockIdx.x & 7, j = blockIdx.x >> 3;
  const int tm = (xc >> 1) * 8 + (j >> 2), tn = (xc & 1) * 4 + (j & 3);
  const int m0 = tm * BM, n0 = tn * BN, b = m0 >> 10;
  const int lane = threadIdx.x & 63, wave = threadIdx.x >> 6, wm = wave % 4, wn = wave / 4;
  const int row = m0 + wm * 16 + (lane & 15);
  const int col0 = n0 + wn * C::WTN + (lane >> 4) * 4;
  f32x4 acc[1][3] = {};
  gemm_main<BM, BN, 4, 2, 6, true, LDA / 64, KI>(A + (size_t)m0 * LDA, LDA, W + n0, DM, smem, acc, NoHook(), NoHook());
  float vals[12];
#pragma unroll
  for (int n = 0; n < 3; ++n) {
    u32x2 xv = *(const u32x2*)(xin + (size_t)row * DM + col0 + n * 16);
    f32x4 gt = *(const f32x4*)(gates + b * (2 * DM) + col0 + n * 16);
    unsigned a0 = pack2(acc[0][n][0], acc[0][n][1]), a1 = pack2(acc[0][n][2], acc[0][n][3]);
    unsigned g0 = pack2(gt[0] * bflo(a0), gt[1] * bfhi(a0));
    unsigned g1 = pack2(gt[2] * bflo(a1), gt[3] * bfhi(a1));
    u32x2 o = {pack2(bflo(xv[0]) + bflo(g0), bfhi(xv[0]) + bfhi(g0)),
               pack2(bflo(xv[1]) + bflo(g1), bfhi(xv[1]) + bfhi(g1))};
    *(u32x2*)(xo + (size_t)row * DM + col0 + n * 16) = o;
    vals[n * 4 + 0] = bflo(o[0]); vals[n * 4 + 1] = bfhi(o[0]);
    vals[n * 4 + 2] = bflo(o[1]); vals[n * 4 + 3] = bfhi(o[1]);
  }
  if (STATS) {
    float s = 0.f;
#pragma unroll
    for (int i = 0; i < 12; ++i) s += vals[i];
    s += __shfl_xor(s, 16);
    s += __shfl_xor(s, 32);
    const float lm = s * (1.f / 48.f);
    float q = 0.f;
#pragma unroll
    for (int i = 0; i < 12; ++i) { float d = vals[i] - lm; q += d * d; }
    q += __shfl_xor(q, 16);
    q += __shfl_xor(q, 32);
    if (lane < 16) pst[(size_t)row * 16 + tn * 2 + wn] = f32x2{s, q};
  }
  STAMP(KI, 3);
}
__global__ __launch_bounds__(512, 2) void k_wo(const u16* __restrict__ O, const u16* __restrict__ wo,
                                               const u16* __restrict__ xin, const float* __restrict__ gates,
                                               u16* __restrict__ x1, f32x2* __restrict__ pst) {
  extern __shared__ __attribute__((aligned(16))) char smem[];
  res_gemm<3, DM, false>(O, wo, xin, gates, x1, pst, smem);
}
__global__ __launch_bounds__(512, 2) void k_fc2(const u16* __restrict__ f, const u16* __restrict__ w2,
                                                const u16* __restrict__ x1, const float* __restrict__ gates,
                                                u16* __restrict__ out) {
  extern __shared__ __attribute__((aligned(16))) char smem[];
  res_gemm<5, FF, false>(f, w2, x1, gates + DM, out, nullptr, smem);
}

__device__ __forceinline__ float gelu_tanh(float u) {
  float w = u * (-2.f * 0.7978845608028654f * 1.4426950408889634f);
  float a = w * (1.f + 0.044715f * u * u);
  return u * __builtin_amdgcn_rcpf(1.f + __builtin_amdgcn_exp2f(a));
}
using GFC = GC<128, 192, 2, 4, 4, true, false>;
__global__ __launch_bounds__(512, 2) void k_fc1(const u16* __restrict__ h2, const u16* __restrict__ w1,
                                                u16* __restrict__ f) {
  extern __shared__ __attribute__((aligned(16))) char smem[];
  using C = GFC;
  constexpr int BM = 128, BN = 192;
  const int xc = blockIdx.x & 7, j = blockIdx.x >> 3;
  const int tm = (xc >> 2) * 8 + (j >> 2), tn = (xc & 3) * 4 + (j & 3);
  const int m0 = tm * BM, n0 = tn * BN;
  f32x4 acc[C::MT][C::NT] = {};
  gemm_main<BM, BN, 2, 4, 4, true, 12, 4>(h2 + (size_t)m0 * DM, DM, w1 + n0, FF, smem, acc, NoHook(), NoHook());
  const int lane = threadIdx.x & 63, wave = threadIdx.x >> 6, wm = wave % 2, wn = wave / 2;
#pragma unroll
  for (int m = 0; m < C::MT; ++m) {
    const int row = m0 + wm * C::WTM + m * 16 + (lane & 15);
    u16* rp = f + (size_t)row * FF + n0 + wn * C::WTN;
    u32x2 v[C::NT];
#pragma unroll
    for (int n = 0; n < C::NT; ++n) {
      unsigned p0 = pack2(acc[m][n][0], acc[m][n][1]), p1 = pack2(acc[m][n][2], acc[m][n][3]);
      v[n] = u32x2{pack2(gelu_tanh(bflo(p0)), gelu_tanh(bfhi(p0))), pack2(gelu_tanh(bflo(p1)), gelu_tanh(bfhi(p1)))};
    }
    store_pair(rp, v[0], v[1]);
    *(u32x2*)(rp + 32 + (lane >> 4) * 4) = v[2];
  }
  STAMP(4, 3);
}

extern "C" void kernel_launch(void* const* d_in, const int* in_sizes, int n_in, void* d_out, int out_size,
                              void* d_ws, size_t ws_size, hipStream_t stream) {
  const u16* x = (const u16*)d_in[0];
  const u16* c = (const u16*)d_in[1];
  const u16* wqkv = (const u16*)d_in[2];
  const u16* wo = (const u16*)d_in[3];
  const u16* wmod = (const u16*)d_in[4];
  const u16* w1 = (const u16*)d_in[5];
  const u16* w2 = (const u16*)d_in[6];
  u16* out = (u16*)d_out;

  char* ws = (char*)d_ws;
  size_t o = 0;
  auto take = [&](size_t bytes) { char* p = ws + o; o += (bytes + 255) & ~(size_t)255; return p; };
  float* gates = (float*)take(2 * 2 * DM * 4);
  float* tab = (float*)take(2 * 2 * 2 * DM * 4);
  u16* h = (u16*)take((size_t)MROWS * DM * 2);
  u16* h2 = (u16*)take((size_t)MROWS * DM * 2);
  u16* qkv = (u16*)take((size_t)MROWS * 3 * DM * 2);
  u16* E = (u16*)take((size_t)4 * NSEQ * NSEQ * 2);
  f32x2* st = (f32x2*)take((size_t)4 * NSEQ * 16 * 8);
  u16* O = (u16*)take((size_t)MROWS * DM * 2);
  u16* x1 = (u16*)take((size_t)MROWS * DM * 2);
  f32x2* pst = (f32x2*)take((size_t)MROWS * 16 * 8);
  u16* f = (u16*)take((size_t)MROWS * FF * 2);

  constexpr int LDS_QKV = GQKV::LDS;
  constexpr int LDS_SC = GSC::LDS;
  constexpr int LDS_PV = GPV::LDS + 16 * 64 * 4;
  constexpr int LDS_WO = GPV::LDS;
  constexpr int LDS_FC = GFC::LDS;
  (void)hipFuncSetAttribute((const void*)k_qkv, hipFuncAttributeMaxDynamicSharedMemorySize, LDS_QKV);
  (void)hipFuncSetAttribute((const void*)k_scores, hipFuncAttributeMaxDynamicSharedMemorySize, LDS_SC);
  (void)hipFuncSetAttribute((const void*)k_pv, hipFuncAttributeMaxDynamicSharedMemorySize, LDS_PV);
  (void)hipFuncSetAttribute((const void*)k_wo, hipFuncAttributeMaxDynamicSharedMemorySize, LDS_WO);
  (void)hipFuncSetAttribute((const void*)k_fc1, hipFuncAttributeMaxDynamicSharedMemorySize, LDS_FC);
  (void)hipFuncSetAttribute((const void*)k_fc2, hipFuncAttributeMaxDynamicSharedMemorySize, LDS_WO);

  k_pre<<<144 + 112, 256, 0, stream>>>(c, wmod, gates, tab, wqkv, wo, w1, w2, (const unsigned*)d_ws, (int)(o >> 12));
  k_lnmod<<<MROWS / 4, 256, 0, stream>>>(x, tab, 0, h);
  k_qkv<<<256, 512, LDS_QKV, stream>>>(h, wqkv, qkv);
  k_scores<<<256, 512, LDS_SC, stream>>>(qkv, E, st);
  k_pv<<<256, 512, LDS_PV, stream>>>(E, st, qkv, O);
  k_wo<<<256, 512, LDS_WO, stream>>>(O, wo, x, gates, x1, pst);
  k_lnmod<<<MROWS / 4, 256, 0, stream>>>(x1, tab, 1, h2);
  k_fc1<<<256, 512, LDS_FC, stream>>>(h2, w1, f);
  k_fc2<<<256, 512, LDS_WO, stream>>>(f, w2, x1, gates, out);
}
```

```cpp
#include <hip/hip_runtime.h>
#include <hip/hip_bf16.h>
#include <stdint.h>

typedef unsigned short u16;
typedef __attribute__((ext_vector_type(8))) __bf16 bf16x8;
typedef __attribute__((ext_vector_type(4))) __bf16 bf16x4;
typedef __attribute__((ext_vector_type(2))) __bf16 bf16x2;
typedef __attribute__((ext_vector_type(4))) float f32x4;
typedef __attribute__((ext_vector_type(2))) float f32x2;
typedef __attribute__((ext_vector_type(4))) unsigned u32x4;
typedef __attribute__((ext_vector_type(2))) unsigned u32x2;
#define LAS __attribute__((address_space(3)))
#define GAS __attribute__((address_space(1)))

constexpr int NB = 2, NSEQ = 1024, DM = 768, NH = 2, DH = 384, FF = 3072;
constexpr int MROWS = NB * NSEQ;

__device__ __forceinline__ float bf2f(u16 v) { return __uint_as_float(((unsigned)v) << 16); }
__device__ __forceinline__ float bflo(unsigned v) { return __uint_as_float(v << 16); }
__device__ __forceinline__ float bfhi(unsigned v) { return __uint_as_float(v & 0xffff0000u); }
__device__ __forceinline__ unsigned pack2(float a, float b) {
  f32x2 f = {a, b};
  bf16x2 r = __builtin_convertvector(f, bf16x2);
  return __builtin_bit_cast(unsigned, r);
}
__device__ __forceinline__ float rbf(float a) {
  return bflo(pack2(a, 0.f) & 0xffffu);
}

template <int N>
__device__ __forceinline__ void waitv() {
  asm volatile("s_waitcnt vmcnt(%0)" ::"i"(N) : "memory");
}
template <int I>
struct IC {
  static constexpr int value = I;
};
template <int B, int E, class F>
__device__ __forceinline__ void static_for(F&& f) {
  if constexpr (B < E) {
    f(IC<B>{});
    static_for<B + 1, E>(f);
  }
}
#define ABL 0
#define FENCE() asm volatile("" ::: "memory")
#define STAMP(kid, i)
#define BARRIER()                        \
  do {                                   \
    FENCE();                             \
    __builtin_amdgcn_s_barrier();        \
    FENCE();                             \
  } while (0)

template <int BM, int BN, int WM, int WN, int NS, bool BKN, bool ATR>
struct GC {
  static constexpr int NW = WM * WN;
  static constexpr int WTM = BM / WM, MT = WTM / 16;
  static constexpr int NTT = BN / 16;
  static constexpr int NT = (NTT + WN - 1) / WN;
  static constexpr int WTN = NT * 16;
  static constexpr int A_BYTES = BM * 128, B_BYTES = BN * 128;
  static constexpr int NSA = ATR ? 3 : NS;
  static constexpr int B_OFF = NSA * A_BYTES;
  static constexpr int TAB_OFF = B_OFF + NS * B_BYTES;
  static constexpr int NA = BM / 8, NBI = BN / 8;
  static constexpr int LA = ATR ? 0 : NA / 4;
  static constexpr int LB = (NBI + 3) / 4;
  static constexpr int NFULL = NBI % 4 == 0 ? 4 : NBI % 4;
  static constexpr int L = LA + LB;
  static constexpr int LDS = TAB_OFF + (ATR ? 8192 : 0);
  static_assert(NW == 8, "8 waves");
  static_assert(NA % 4 == 0, "A instr split");
  static_assert(NS >= 4, "stages");
  static_assert(LDS <= 163840, "lds");
};

template <class C, int BN, int WM, int WN, bool BKN>
struct GemmCtx {
  static constexpr int MT = C::MT, NT = C::NT, L = C::L, LA = C::LA, LB = C::LB;
  const char* Abase;
  const char* Bbase;
  size_t bstep;
  char* smem;
  unsigned goff[L > 0 ? L : 1];
  unsigned aoff[2], boff[2];
  int wq, par, last_n;
  bool full;

  __device__ __forceinline__ void init(const u16* Ab, int lda, const u16* Bb, int ldb, char* sm) {
    const int tid = threadIdx.x, lane = tid & 63;
    const int wave = __builtin_amdgcn_readfirstlane(tid >> 6);
    const int wm = wave % WM, wn = wave / WM;
    wq = wave & 3;
    par = wave >> 2;
    full = (C::NFULL == 4) || (wq < C::NFULL);
    smem = sm;
#pragma unroll
    for (int i = 0; i < LA; ++i) {
      int r = i * 4 + wq;
      int row = r * 8 + (lane >> 3);
      int lc = (lane & 7) ^ (row & 7);
      goff[i] = (unsigned)(row * lda + lc * 8) * 2u;
    }
#pragma unroll
    for (int i = 0; i < LB; ++i) {
      int r = i * 4 + wq;
      if (r >= C::NBI) r = 0;
      if (BKN) {
        int st = r * 4 + (lane >> 4);
        int kb = st / (BN / 16), nb = st % (BN / 16);
        int i16 = lane & 15, prow = i16 >> 1, hf = i16 & 1;
        int lrow = prow ^ ((kb & 1) << 2);
        goff[LA + i] = (unsigned)((kb * 8 + lrow) * ldb + nb * 16 + hf * 8) * 2u;
      } else {
        int row = r * 8 + (lane >> 3);
        int lc = (lane & 7) ^ (row & 7);
        goff[LA + i] = (unsigned)(row * ldb + lc * 8) * 2u;
      }
    }
    Abase = (const char*)Ab;
    Bbase = (const char*)Bb;
    bstep = BKN ? (size_t)64 * ldb * 2 : (size_t)128;
    const int g = lane >> 4, i16 = lane & 15;
#pragma unroll
    for (int s = 0; s < 2; ++s)
      aoff[s] = (unsigned)((wm * C::WTM + i16) * 128 + (((s * 4 + g) ^ (lane & 7)) << 4));
    if (BKN) {
      const int q4 = i16 >> 2, p = i16 & 3;
#pragma unroll
      for (int h = 0; h < 2; ++h)
        boff[h] = (unsigned)(C::B_OFF + (g * (BN / 16) + wn * NT) * 256 + (((h * 4 + q4) ^ ((g & 1) << 2)) * 32) +
                             p * 8);
    } else {
#pragma unroll
      for (int s = 0; s < 2; ++s)
        boff[s] = (unsigned)(C::B_OFF + (wn * C::WTN + i16) * 128 + (((s * 4 + g) ^ (lane & 7)) << 4));
    }
    const bool last_dummy = (C::NTT % WN != 0) && (wn * NT + NT > C::NTT);
    last_n = last_dummy ? 0 : NT - 1;
  }
  template <int i>
  __device__ __forceinline__ void issue1(int t, int sl) {
    if constexpr (i < LA) {
      __builtin_amdgcn_global_load_lds((const GAS unsigned*)(Abase + (size_t)t * 128 + goff[i]),
                                       (LAS unsigned*)(smem + sl * C::A_BYTES + wq * 1024 + i * 4096), 16, 0, 0);
    } else {
      if (i + 1 < L || full)
        __builtin_amdgcn_global_load_lds(
            (const GAS unsigned*)(Bbase + (size_t)t * bstep + goff[i]),
            (LAS unsigned*)(smem + C::B_OFF + sl * C::B_BYTES + wq * 1024 + (i - LA) * 4096), 16, 0, 0);
    }
  }
  __device__ __forceinline__ void issue_all(int t, int sl) {
    static_for<0, L>([&](auto II) { issue1<II.value>(t, sl); });
  }
  template <int n, int x>
  __device__ __forceinline__ void wait_st() {
    if (C::NFULL == 4 || n == 0) {
      waitv<n * L + x>();
    } else {
      if (full) waitv<n * L + x>();
      else waitv<n * (L - 1) + x>();
    }
  }
  template <int s>
  __device__ __forceinline__ void rd_frag(int sla, int slb, bf16x8 (&fa)[MT], bf16x8 (&fb)[NT]) {
    const char* sa = smem + sla * C::A_BYTES;
    const char* sb = smem + slb * C::B_BYTES;
#pragma unroll
    for (int m = 0; m < MT; ++m) fa[m] = *(const bf16x8*)(sa + aoff[s] + m * 2048);
#pragma unroll
    for (int n = 0; n < NT; ++n) {
      const int nn = (n == NT - 1) ? last_n : n;
      if (BKN) {
        bf16x4 lo = __builtin_amdgcn_ds_read_tr16_b64_v4bf16(
            (LAS bf16x4*)(sb + boff[0] + s * 4 * (BN / 16) * 256 + nn * 256));
        bf16x4 hi = __builtin_amdgcn_ds_read_tr16_b64_v4bf16(
            (LAS bf16x4*)(sb + boff[1] + s * 4 * (BN / 16) * 256 + nn * 256));
        fb[n] = __builtin_shufflevector(lo, hi, 0, 1, 2, 3, 4, 5, 6, 7);
      } else {
        fb[n] = *(const bf16x8*)(sb + boff[s] + nn * 2048);
      }
    }
  }
  template <bool ISS>
  __device__ __forceinline__ void mma(f32x4 (&acc)[MT][NT], bf16x8 (&fa)[MT], bf16x8 (&fb)[NT], int ti, int sl,
                                      bool do_issue) {
    static_for<0, MT * NT>([&](auto Q) {
      constexpr int q = Q.value, m = q / NT, n = q % NT;
      acc[m][n] = __builtin_amdgcn_mfma_f32_16x16x32_bf16(fb[n], fa[m], acc[m][n], 0, 0, 0);
      if constexpr (ISS) {
        constexpr int lo = (q * L) / (MT * NT), hi = ((q + 1) * L) / (MT * NT);
        if constexpr (hi > lo) {
          if (do_issue) static_for<lo, hi>([&](auto II) { issue1<II.value>(ti, sl); });
          __builtin_amdgcn_sched_barrier(0);
        }
      }
    });
  }
};

template <int BM, int BN, int WM, int WN, int NS, bool BKN, int T, int KID, class Pre, class Hook>
__device__ __forceinline__ void gemm_main(const u16* __restrict__ Ab, int lda, const u16* __restrict__ Bb, int ldb,
                                          char* smem,
                                          f32x4 (&acc)[GC<BM, BN, WM, WN, NS, BKN, false>::MT][GC<BM, BN, WM, WN, NS, BKN, false>::NT],
                                          Pre pre, Hook hook) {
  using C = GC<BM, BN, WM, WN, NS, BKN, false>;
  constexpr int MT = C::MT, NT = C::NT;
  GemmCtx<C, BN, WM, WN, BKN> g;
  g.init(Ab, lda, Bb, ldb, smem);
  STAMP(KID, 0);
  static_for<0, NS - 1>([&](auto S) {
    if ((S.value & 1) == g.par) g.issue_all(S.value, S.value);
  });
  pre();
  if (g.par == 0) g.template wait_st<(NS - 2) / 2, 0>();
  BARRIER();
  STAMP(KID, 1);
  bf16x8 fa0[MT], fb0[NT], fa1[MT], fb1[NT];
  g.template rd_frag<0>(0, 0, fa0, fb0);
  constexpr int U = (NS % 2 == 0) ? NS : 2 * NS;
  constexpr int NSTEADY = T - NS + 1;
  constexpr int NBLK = NSTEADY / U;
#pragma unroll 1
  for (int tb = 0; tb < NBLK * U; tb += U) {
    static_for<0, U>([&](auto UU) {
      constexpr int u = UU.value;
      const int t = tb + u;
      g.template rd_frag<1>(u % NS, u % NS, fa1, fb1);
      __builtin_amdgcn_sched_barrier(0);
      g.template mma<false>(acc, fa0, fb0, 0, 0, false);
      __builtin_amdgcn_sched_barrier(0);
      if (g.par == ((u + 1) & 1)) g.template wait_st<(NS - 3) / 2, 0>();
      BARRIER();
      g.template rd_frag<0>((u + 1) % NS, (u + 1) % NS, fa0, fb0);
      __builtin_amdgcn_sched_barrier(0);
      g.template mma<true>(acc, fa1, fb1, t + NS - 1, (u + NS - 1) % NS, g.par == ((u + NS - 1) & 1));
      hook(t);
      __builtin_amdgcn_sched_barrier(0);
    });
  }
  static_for<NBLK * U, NSTEADY>([&](auto TT) {
    constexpr int t = TT.value;
    g.template rd_frag<1>(t % NS, t % NS, fa1, fb1);
    __builtin_amdgcn_sched_barrier(0);
    g.template mma<false>(acc, fa0, fb0, 0, 0, false);
    __builtin_amdgcn_sched_barrier(0);
    if (g.par == ((t + 1) & 1)) g.template wait_st<(NS - 3) / 2, 0>();
    BARRIER();
    g.template rd_frag<0>((t + 1) % NS, (t + 1) % NS, fa0, fb0);
    __builtin_amdgcn_sched_barrier(0);
    g.template mma<true>(acc, fa1, fb1, t + NS - 1, (t + NS - 1) % NS, g.par == ((t + NS - 1) & 1));
    hook(t);
    __builtin_amdgcn_sched_barrier(0);
  });
  static_for<T - NS + 1, T>([&](auto TT) {
    constexpr int t = TT.value;
    g.template rd_frag<1>(t % NS, t % NS, fa1, fb1);
    __builtin_amdgcn_sched_barrier(0);
    g.template mma<false>(acc, fa0, fb0, 0, 0, false);
    __builtin_amdgcn_sched_barrier(0);
    if constexpr (t + 1 < T) {
      constexpr int younger = (T - 1) >= t + 3 ? (T - 1 - (t + 1)) / 2 : 0;
      if (g.par == ((t + 1) & 1)) g.template wait_st<younger, 0>();
      BARRIER();
      g.template rd_frag<0>((t + 1) % NS, (t + 1) % NS, fa0, fb0);
      __builtin_amdgcn_sched_barrier(0);
    }
    g.template mma<false>(acc, fa1, fb1, 0, 0, false);
    hook(t);
    __builtin_amdgcn_sched_barrier(0);
  });
  STAMP(KID, 2);
}

__device__ __forceinline__ void store_pair(u16* rowp, u32x2 a, u32x2 b) {
  auto rx = __builtin_amdgcn_permlane16_swap(a[0], b[0], false, false);
  auto ry = __builtin_amdgcn_permlane16_swap(a[1], b[1], false, false);
  const int g = (threadIdx.x & 63) >> 4;
  u32x4 v = {rx[0], ry[0], rx[1], ry[1]};
  *(u32x4*)(rowp + (g & 1) * 16 + (g >> 1) * 8) = v;
}

struct NoHook {
  __device__ __forceinline__ void operator()(int) const {}
  __device__ __forceinline__ void operator()() const {}
};

__device__ __forceinline__ f32x2 ln_params(float mean, float var) {
  const float mb = rbf(mean), vb = rbf(var);
  const float sd = rbf(__fsqrt_rn(rbf(vb + 1e-5f)));
  const float ra = 1.f / sd;
  return f32x2{ra, -mb * ra};
}
__global__ __launch_bounds__(512) void k_pre(const u16* __restrict__ c, const u16* __restrict__ wmod,
                                             float* __restrict__ gates, float* __restrict__ tab,
                                             const u16* __restrict__ pw0) {
  __shared__ float red[2][128][33];
  __shared__ float sil[2][DM];
  const int tid = threadIdx.x;
  STAMP(6, 0); STAMP(6, 1); STAMP(6, 2);
  if (blockIdx.x < 144) {
    const int cg = tid & 3, kq = tid >> 2;
    const int col0 = blockIdx.x * 32 + cg * 8;
    u32x4 w[6];
#pragma unroll
    for (int i = 0; i < 6; ++i) {
      int k = i * 128 + kq;
      w[i] = *(const u32x4*)(wmod + (size_t)k * (6 * DM) + col0);
    }
#pragma unroll
    for (int i = 0; i < 3; ++i) {
      int idx = i * 512 + tid;
      float cv = bf2f(c[idx]);
      (&sil[0][0])[idx] = rbf(cv / (1.f + __expf(-cv)));
    }
    __syncthreads();
    float a0[8], a1[8];
#pragma unroll
    for (int j = 0; j < 8; ++j) a0[j] = a1[j] = 0.f;
#pragma unroll
    for (int i = 0; i < 6; ++i) {
      int k = i * 128 + kq;
      float s0 = sil[0][k], s1 = sil[1][k];
#pragma unroll
      for (int j = 0; j < 4; ++j) {
        float wl = bflo(w[i][j]), wh = bfhi(w[i][j]);
        a0[2 * j] += s0 * wl; a0[2 * j + 1] += s0 * wh;
        a1[2 * j] += s1 * wl; a1[2 * j + 1] += s1 * wh;
      }
    }
#pragma unroll
    for (int j = 0; j < 8; ++j) {
      red[0][kq][cg * 8 + j] = a0[j];
      red[1][kq][cg * 8 + j] = a1[j];
    }
    __syncthreads();
    {
      const int o = tid >> 3, part = tid & 7;
      const int b = o >> 5, cl = o & 31;
      float s = 0.f;
#pragma unroll
      for (int q = 0; q < 16; ++q) s += red[b][part * 16 + q][cl];
      s += __shfl_xor(s, 1);
      s += __shfl_xor(s, 2);
      s += __shfl_xor(s, 4);
      if (part == 0) {
        const float v = rbf(s);
        const int col = blockIdx.x * 32 + cl, i = col / DM, cc = col % DM;
        const int w_ = i / 3, kind = i % 3;
        if (kind == 2) gates[(b * 2 + w_) * DM + cc] = v;
        else if (kind == 1) tab[((b * 2 + w_) * 2 + 0) * DM + cc] = rbf(1.f + v);
        else tab[((b * 2 + w_) * 2 + 1) * DM + cc] = v;
      }
    }
  } else {
    const int wb = blockIdx.x - 144;
    constexpr int N0 = DM * 3 * DM / 64;
    unsigned vv[4];
#pragma unroll
    for (int q = 0; q < 4; ++q) {
      int l = (q * 14 + wb) * 512 + tid;
      l = l < N0 ? l : 0;
      vv[q] = *(const unsigned*)(pw0 + (size_t)l * 64);
    }
    unsigned accu = vv[0] + vv[1] + vv[2] + vv[3];
    if (accu == 0x9e3779b9u && tid == 999) gates[0] = 0.f;
  }
  STAMP(6, 3);
}

__global__ __launch_bounds__(256) void k_lnmod(const u16* __restrict__ x, const float* __restrict__ tab, int which,
                                               u16* __restrict__ out, const u16* __restrict__ pw1,
                                               const u16* __restrict__ pw2, const u16* __restrict__ pw3) {
  if (blockIdx.x >= MROWS / 4) {
    constexpr int N1 = DM * DM / 64, N2 = N1 + DM * FF / 64, N3 = N2 + FF * DM / 64;
    const int wb = blockIdx.x - MROWS / 4;
    unsigned vv[4];
#pragma unroll
    for (int q = 0; q < 4; ++q) {
      int l = (q * 81 + wb) * 256 + threadIdx.x;
      l = l < N3 ? l : 0;
      const u16* p = l < N1 ? pw1 + (size_t)l * 64 : (l < N2 ? pw2 + (size_t)(l - N1) * 64 : pw3 + (size_t)(l - N2) * 64);
      vv[q] = *(const unsigned*)p;
    }
    if (vv[0] + vv[1] + vv[2] + vv[3] == 0x9e3779b9u && threadIdx.x == 999) out[0] = 0;
    return;
  }
  const int lane = threadIdx.x & 63, row = blockIdx.x * 4 + (threadIdx.x >> 6);
  const int b = row >> 10;
  if (which == 0) { STAMP(7, 0); STAMP(7, 1); STAMP(7, 2); }
  const u16* xr = x + (size_t)row * DM;
  float v[12];
#pragma unroll
  for (int i = 0; i < 3; ++i) {
    u32x2 u = *(const u32x2*)(xr + i * 256 + lane * 4);
    v[i * 4 + 0] = bflo(u[0]); v[i * 4 + 1] = bfhi(u[0]);
    v[i * 4 + 2] = bflo(u[1]); v[i * 4 + 3] = bfhi(u[1]);
  }
  const float* S = tab + (size_t)(b * 2 + which) * 2 * DM;
  const float* Tt = S + DM;
  f32x4 sc[3], sh[3];
#pragma unroll
  for (int i = 0; i < 3; ++i) {
    sc[i] = *(const f32x4*)(S + i * 256 + lane * 4);
    sh[i] = *(const f32x4*)(Tt + i * 256 + lane * 4);
  }
  float s = 0.f;
#pragma unroll
  for (int i = 0; i < 12; ++i) s += v[i];
#pragma unroll
  for (int o = 32; o; o >>= 1) s += __shfl_xor(s, o);
  const float mean = s * (1.f / DM);
  float q = 0.f;
#pragma unroll
  for (int i = 0; i < 12; ++i) { float d = v[i] - mean; q += d * d; }
#pragma unroll
  for (int o = 32; o; o >>= 1) q += __shfl_xor(q, o);
  const float mb = rbf(mean), vb = rbf(q * (1.f / DM));
  const float sd = rbf(__fsqrt_rn(rbf(vb + 1e-5f)));
#pragma unroll
  for (int i = 0; i < 3; ++i) {
    float h[4];
#pragma unroll
    for (int j = 0; j < 4; ++j) {
      float n = rbf(rbf(v[i * 4 + j] - mb) / sd);
      h[j] = rbf(n * sc[i][j]) + sh[i][j];
    }
    u32x2 o = {pack2(h[0], h[1]), pack2(h[2], h[3])};
    *(u32x2*)(out + (size_t)row * DM + i * 256 + lane * 4) = o;
  }
  if (which == 0) { STAMP(7, 3); }
}

using GQKV = GC<128, 144, 4, 2, 4, true, false>;
__global__ __launch_bounds__(512, 2) void k_qkv(const u16* __restrict__ h, const u16* __restrict__ w,
                                                u16* __restrict__ qkv) {
  extern __shared__ __attribute__((aligned(16))) char smem[];
  using C = GQKV;
  constexpr int BM = 128, BN = 144;
  const int xc = blockIdx.x & 7, j = blockIdx.x >> 3;
  const int tm = (xc >> 2) * 8 + (j >> 2), tn = (xc & 3) * 4 + (j & 3);
  const int m0 = tm * BM, n0 = tn * BN;
  f32x4 acc[C::MT][C::NT] = {};
  gemm_main<BM, BN, 4, 2, 4, true, 12, 0>(h + (size_t)m0 * DM, DM, w + n0, 3 * DM, smem, acc, NoHook(), NoHook());
  const int lane = threadIdx.x & 63, wave = threadIdx.x >> 6, wm = wave % 4, wn = wave / 4;
#pragma unroll
  for (int m = 0; m < C::MT; ++m) {
    const int row = m0 + wm * C::WTM + m * 16 + (lane & 15);
    u16* rp = qkv + (size_t)row * (3 * DM) + n0 + wn * C::WTN;
    u32x2 v[C::NT];
#pragma unroll
    for (int n = 0; n < C::NT; ++n)
      v[n] = u32x2{pack2(acc[m][n][0], acc[m][n][1]), pack2(acc[m][n][2], acc[m][n][3])};
    store_pair(rp, v[0], v[1]);
    store_pair(rp + 32, v[2], v[3]);
    if (wn == 0) *(u32x2*)(rp + 64 + (lane >> 4) * 4) = v[4];
  }
  STAMP(0, 3);
}

using GSC = GC<128, 128, 4, 2, 4, false, false>;
__global__ __launch_bounds__(512, 2) void k_scores(const u16* __restrict__ qkv, u16* __restrict__ E,
                                                   f32x2* __restrict__ st) {
  extern __shared__ __attribute__((aligned(16))) char smem[];
  using C = GSC;
  constexpr int BM = 128, BN = 128;
  const int xc = blockIdx.x & 7, j = blockIdx.x >> 3;
  const int bh = xc >> 1, b = bh >> 1, hh = bh & 1;
  const int tm = (xc & 1) * 4 + (j >> 3), tn = j & 7;
  const int m0 = tm * BM, n0 = tn * BN;
  const u16* Q = qkv + (size_t)(b * NSEQ + m0) * (3 * DM) + hh * DH;
  const u16* K = qkv + (size_t)(b * NSEQ + n0) * (3 * DM) + DM + hh * DH;
  f32x4 acc[C::MT][C::NT] = {};
  gemm_main<BM, BN, 4, 2, 4, false, 6, 1>(Q, 3 * DM, K, 3 * DM, smem, acc, NoHook(), NoHook());
  const int lane = threadIdx.x & 63, wave = threadIdx.x >> 6, wm = wave % 4, wn = wave / 4;
  const float scale = 0.05103103630798288f;
#pragma unroll
  for (int m = 0; m < C::MT; ++m) {
    float s[C::NT][4];
    float mx = -INFINITY;
#pragma unroll
    for (int n = 0; n < C::NT; ++n) {
      unsigned p0 = pack2(acc[m][n][0], acc[m][n][1]), p1 = pack2(acc[m][n][2], acc[m][n][3]);
      p0 = pack2(bflo(p0) * scale, bfhi(p0) * scale);
      p1 = pack2(bflo(p1) * scale, bfhi(p1) * scale);
      s[n][0] = bflo(p0); s[n][1] = bfhi(p0); s[n][2] = bflo(p1); s[n][3] = bfhi(p1);
      mx = fmaxf(mx, fmaxf(fmaxf(s[n][0], s[n][1]), fmaxf(s[n][2], s[n][3])));
    }
    mx = fmaxf(mx, __shfl_xor(mx, 16));
    mx = fmaxf(mx, __shfl_xor(mx, 32));
    float sum = 0.f;
    const int row = m0 + wm * C::WTM + m * 16 + (lane & 15);
    u16* Er = E + ((size_t)bh * NSEQ + row) * NSEQ + n0 + wn * C::WTN;
    u32x2 v[C::NT];
#pragma unroll
    for (int n = 0; n < C::NT; ++n) {
#pragma unroll
      for (int e = 0; e < 4; ++e) {
        s[n][e] = __expf(s[n][e] - mx);
        sum += s[n][e];
      }
      v[n] = u32x2{pack2(s[n][0], s[n][1]), pack2(s[n][2], s[n][3])};
    }
    store_pair(Er, v[0], v[1]);
    store_pair(Er + 32, v[2], v[3]);
    sum += __shfl_xor(sum, 16);
    sum += __shfl_xor(sum, 32);
    if (lane < 16) st[((size_t)bh * NSEQ + row) * 16 + tn * 2 + wn] = f32x2{mx, sum};
  }
  STAMP(1, 3);
}

using GPV = GC<64, 96, 4, 2, 6, true, false>;
__global__ __launch_bounds__(512, 2) void k_pv(const u16* __restrict__ E, const f32x2* __restrict__ st,
                                               const u16* __restrict__ qkv, u16* __restrict__ O) {
  extern __shared__ __attribute__((aligned(16))) char smem[];
  using C = GPV;
  constexpr int BM = 64, BN = 96;
  const int xc = blockIdx.x & 7, j = blockIdx.x >> 3;
  const int bh = xc >> 1, b = bh >> 1, hh = bh & 1;
  const int tm = (xc & 1) * 8 + (j >> 2), tn = j & 3;
  const int m0 = tm * BM, n0 = tn * BN;
  const int lane = threadIdx.x & 63, wave = threadIdx.x >> 6, wm = wave % 4, wn = wave / 4;
  float* ftab = (float*)(smem + C::LDS);
  auto pre = [&]() {
    if (threadIdx.x < 64) {
      const f32x4* p = (const f32x4*)(st + ((size_t)bh * NSEQ + m0 + threadIdx.x) * 16);
      f32x4 v[8];
#pragma unroll
      for (int t = 0; t < 8; ++t) v[t] = p[t];
      float M = -INFINITY;
#pragma unroll
      for (int t = 0; t < 8; ++t) M = fmaxf(M, fmaxf(v[t][0], v[t][2]));
      float fac[16], Ls = 0.f;
#pragma unroll
      for (int t = 0; t < 8; ++t) {
        fac[2 * t] = __expf(v[t][0] - M);
        fac[2 * t + 1] = __expf(v[t][2] - M);
        Ls += fac[2 * t] * v[t][1] + fac[2 * t + 1] * v[t][3];
      }
      float inv = 1.f / Ls;
#pragma unroll
      for (int t = 0; t < 16; ++t) ftab[t * 64 + threadIdx.x] = fac[t] * inv;
      asm volatile("s_waitcnt lgkmcnt(0)" ::: "memory");
    }
  };
  const u16* A = E + ((size_t)bh * NSEQ + m0) * NSEQ;
  const u16* V = qkv + (size_t)(b * NSEQ) * (3 * DM) + 2 * DM + hh * DH + n0;
  f32x4 acc[1][3] = {};
  f32x4 tot[1][3] = {};
  const float* frow = ftab + wm * 16 + (lane & 15);
  auto hook = [&](int t) {
    const float f = frow[t * 64];
#pragma unroll
    for (int n = 0; n < 3; ++n) {
      tot[0][n] += acc[0][n] * f;
      acc[0][n] = f32x4{0.f, 0.f, 0.f, 0.f};
    }
  };
  gemm_main<BM, BN, 4, 2, 6, true, 16, 2>(A, NSEQ, V, 3 * DM, smem, acc, pre, hook);
#pragma unroll
  for (int n = 0; n < 3; ++n) {
    int row = b * NSEQ + m0 + wm * 16 + (lane & 15);
    int col = hh * DH + n0 + wn * C::WTN + n * 16 + (lane >> 4) * 4;
    u32x2 v = {pack2(tot[0][n][0], tot[0][n][1]), pack2(tot[0][n][2], tot[0][n][3])};
    *(u32x2*)(O + (size_t)row * DM + col) = v;
  }
  STAMP(2, 3);
}

template <int KI, int LDA, bool STATS>
__device__ __forceinline__ void res_gemm(const u16* __restrict__ A, const u16* __restrict__ W,
                                         const u16* __restrict__ xin, const float* __restrict__ gates,
                                         u16* __restrict__ xo, f32x2* __restrict__ pst, char* smem) {
  using C = GPV;
  constexpr int BM = 64, BN = 96;
  const int xc = blockIdx.x & 7, j = blockIdx.x >> 3;
  const int tm = (xc >> 1) * 8 + (j >> 2), tn = (xc & 1) * 4 + (j & 3);
  const int m0 = tm * BM, n0 = tn * BN, b = m0 >> 10;
  const int lane = threadIdx.x & 63, wave = threadIdx.x >> 6, wm = wave % 4, wn = wave / 4;
  const int row = m0 + wm * 16 + (lane & 15);
  const int col0 = n0 + wn * C::WTN + (lane >> 4) * 4;
  f32x4 acc[1][3] = {};
  gemm_main<BM, BN, 4, 2, 6, true, LDA / 64, KI>(A + (size_t)m0 * LDA, LDA, W + n0, DM, smem, acc, NoHook(), NoHook());
  float vals[12];
#pragma unroll
  for (int n = 0; n < 3; ++n) {
    u32x2 xv = *(const u32x2*)(xin + (size_t)row * DM + col0 + n * 16);
    f32x4 gt = *(const f32x4*)(gates + b * (2 * DM) + col0 + n * 16);
    unsigned a0 = pack2(acc[0][n][0], acc[0][n][1]), a1 = pack2(acc[0][n][2], acc[0][n][3]);
    unsigned g0 = pack2(gt[0] * bflo(a0), gt[1] * bfhi(a0));
    unsigned g1 = pack2(gt[2] * bflo(a1), gt[3] * bfhi(a1));
    u32x2 o = {pack2(bflo(xv[0]) + bflo(g0), bfhi(xv[0]) + bfhi(g0)),
               pack2(bflo(xv[1]) + bflo(g1), bfhi(xv[1]) + bfhi(g1))};
    *(u32x2*)(xo + (size_t)row * DM + col0 + n * 16) = o;
    vals[n * 4 + 0] = bflo(o[0]); vals[n * 4 + 1] = bfhi(o[0]);
    vals[n * 4 + 2] = bflo(o[1]); vals[n * 4 + 3] = bfhi(o[1]);
  }
  if (STATS) {
    float s = 0.f;
#pragma unroll
    for (int i = 0; i < 12; ++i) s += vals[i];
    s += __shfl_xor(s, 16);
    s += __shfl_xor(s, 32);
    const float lm = s * (1.f / 48.f);
    float q = 0.f;
#pragma unroll
    for (int i = 0; i < 12; ++i) { float d = vals[i] - lm; q += d * d; }
    q += __shfl_xor(q, 16);
    q += __shfl_xor(q, 32);
    if (lane < 16) pst[(size_t)row * 16 + tn * 2 + wn] = f32x2{s, q};
  }
  STAMP(KI, 3);
}
__global__ __launch_bounds__(512, 2) void k_wo(const u16* __restrict__ O, const u16* __restrict__ wo,
                                               const u16* __restrict__ xin, const float* __restrict__ gates,
                                               u16* __restrict__ x1, f32x2* __restrict__ pst) {
  extern __shared__ __attribute__((aligned(16))) char smem[];
  res_gemm<3, DM, false>(O, wo, xin, gates, x1, pst, smem);
}
__global__ __launch_bounds__(512, 2) void k_fc2(const u16* __restrict__ f, const u16* __restrict__ w2,
                                                const u16* __restrict__ x1, const float* __restrict__ gates,
                                                u16* __restrict__ out) {
  extern __shared__ __attribute__((aligned(16))) char smem[];
  res_gemm<5, FF, false>(f, w2, x1, gates + DM, out, nullptr, smem);
}

__device__ __forceinline__ float gelu_tanh(float u) {
  float w = u * (-2.f * 0.7978845608028654f * 1.4426950408889634f);
  float a = w * (1.f + 0.044715f * u * u);
  return u * __builtin_amdgcn_rcpf(1.f + __builtin_amdgcn_exp2f(a));
}
using GFC = GC<128, 192, 2, 4, 4, true, false>;
__global__ __launch_bounds__(512, 2) void k_fc1(const u16* __restrict__ h2, const u16* __restrict__ w1,
                                                u16* __restrict__ f) {
  extern __shared__ __attribute__((aligned(16))) char smem[];
  using C = GFC;
  constexpr int BM = 128, BN = 192;
  const int xc = blockIdx.x & 7, j = blockIdx.x >> 3;
  const int tm = (xc >> 2) * 8 + (j >> 2), tn = (xc & 3) * 4 + (j & 3);
  const int m0 = tm * BM, n0 = tn * BN;
  f32x4 acc[C::MT][C::NT] = {};
  gemm_main<BM, BN, 2, 4, 4, true, 12, 4>(h2 + (size_t)m0 * DM, DM, w1 + n0, FF, smem, acc, NoHook(), NoHook());
  const int lane = threadIdx.x & 63, wave = threadIdx.x >> 6, wm = wave % 2, wn = wave / 2;
#pragma unroll
  for (int m = 0; m < C::MT; ++m) {
    const int row = m0 + wm * C::WTM + m * 16 + (lane & 15);
    u16* rp = f + (size_t)row * FF + n0 + wn * C::WTN;
    u32x2 v[C::NT];
#pragma unroll
    for (int n = 0; n < C::NT; ++n) {
      unsigned p0 = pack2(acc[m][n][0], acc[m][n][1]), p1 = pack2(acc[m][n][2], acc[m][n][3]);
      v[n] = u32x2{pack2(gelu_tanh(bflo(p0)), gelu_tanh(bfhi(p0))), pack2(gelu_tanh(bflo(p1)), gelu_tanh(bfhi(p1)))};
    }
    store_pair(rp, v[0], v[1]);
    *(u32x2*)(rp + 32 + (lane >> 4) * 4) = v[2];
  }
  STAMP(4, 3);
}

extern "C" void kernel_launch(void* const* d_in, const int* in_sizes, int n_in, void* d_out, int out_size,
                              void* d_ws, size_t ws_size, hipStream_t stream) {
  const u16* x = (const u16*)d_in[0];
  const u16* c = (const u16*)d_in[1];
  const u16* wqkv = (const u16*)d_in[2];
  const u16* wo = (const u16*)d_in[3];
  const u16* wmod = (const u16*)d_in[4];
  const u16* w1 = (const u16*)d_in[5];
  const u16* w2 = (const u16*)d_in[6];
  u16* out = (u16*)d_out;

  char* ws = (char*)d_ws;
  size_t o = 0;
  auto take = [&](size_t bytes) { char* p = ws + o; o += (bytes + 255) & ~(size_t)255; return p; };
  float* gates = (float*)take(2 * 2 * DM * 4);
  float* tab = (float*)take(2 * 2 * 2 * DM * 4);
  u16* h = (u16*)take((size_t)MROWS * DM * 2);
  u16* h2 = (u16*)take((size_t)MROWS * DM * 2);
  u16* qkv = (u16*)take((size_t)MROWS * 3 * DM * 2);
  u16* E = (u16*)take((size_t)4 * NSEQ * NSEQ * 2);
  f32x2* st = (f32x2*)take((size_t)4 * NSEQ * 16 * 8);
  u16* O = (u16*)take((size_t)MROWS * DM * 2);
  u16* x1 = (u16*)take((size_t)MROWS * DM * 2);
  f32x2* pst = (f32x2*)take((size_t)MROWS * 16 * 8);
  u16* f = (u16*)take((size_t)MROWS * FF * 2);

  constexpr int LDS_QKV = GQKV::LDS;
  constexpr int LDS_SC = GSC::LDS;
  constexpr int LDS_PV = GPV::LDS + 16 * 64 * 4;
  constexpr int LDS_WO = GPV::LDS;
  constexpr int LDS_FC = GFC::LDS;
  (void)hipFuncSetAttribute((const void*)k_qkv, hipFuncAttributeMaxDynamicSharedMemorySize, LDS_QKV);
  (void)hipFuncSetAttribute((const void*)k_scores, hipFuncAttributeMaxDynamicSharedMemorySize, LDS_SC);
  (void)hipFuncSetAttribute((const void*)k_pv, hipFuncAttributeMaxDynamicSharedMemorySize, LDS_PV);
  (void)hipFuncSetAttribute((const void*)k_wo, hipFuncAttributeMaxDynamicSharedMemorySize, LDS_WO);
  (void)hipFuncSetAttribute((const void*)k_fc1, hipFuncAttributeMaxDynamicSharedMemorySize, LDS_FC);
  (void)hipFuncSetAttribute((const void*)k_fc2, hipFuncAttributeMaxDynamicSharedMemorySize, LDS_WO);

  k_pre<<<144 + 14, 512, 0, stream>>>(c, wmod, gates, tab, wqkv);
  k_lnmod<<<MROWS / 4 + 81, 256, 0, stream>>>(x, tab, 0, h, wo, w1, w2);
  k_qkv<<<256, 512, LDS_QKV, stream>>>(h, wqkv, qkv);
  k_scores<<<256, 512, LDS_SC, stream>>>(qkv, E, st);
  k_pv<<<256, 512, LDS_PV, stream>>>(E, st, qkv, O);
  k_wo<<<256, 512, LDS_WO, stream>>>(O, wo, x, gates, x1, pst);
  k_lnmod<<<MROWS / 4, 256, 0, stream>>>(x1, tab, 1, h2, wo, w1, w2);
  k_fc1<<<256, 512, LDS_FC, stream>>>(h2, w1, f);
  k_fc2<<<256, 512, LDS_WO, stream>>>(f, w2, x1, gates, out);
}
```

```cpp
#include <hip/hip_runtime.h>
#include <hip/hip_bf16.h>
#include <stdint.h>

typedef unsigned short u16;
typedef __attribute__((ext_vector_type(8))) __bf16 bf16x8;
typedef __attribute__((ext_vector_type(4))) __bf16 bf16x4;
typedef __attribute__((ext_vector_type(2))) __bf16 bf16x2;
typedef __attribute__((ext_vector_type(4))) float f32x4;
typedef __attribute__((ext_vector_type(2))) float f32x2;
typedef __attribute__((ext_vector_type(4))) unsigned u32x4;
typedef __attribute__((ext_vector_type(2))) unsigned u32x2;
#define LAS __attribute__((address_space(3)))
#define GAS __attribute__((address_space(1)))

constexpr int NB = 2, NSEQ = 1024, DM = 768, NH = 2, DH = 384, FF = 3072;
constexpr int MROWS = NB * NSEQ;

__device__ __forceinline__ float bf2f(u16 v) { return __uint_as_float(((unsigned)v) << 16); }
__device__ __forceinline__ float bflo(unsigned v) { return __uint_as_float(v << 16); }
__device__ __forceinline__ float bfhi(unsigned v) { return __uint_as_float(v & 0xffff0000u); }
__device__ __forceinline__ unsigned pack2(float a, float b) {
  f32x2 f = {a, b};
  bf16x2 r = __builtin_convertvector(f, bf16x2);
  return __builtin_bit_cast(unsigned, r);
}
__device__ __forceinline__ float rbf(float a) {
  return bflo(pack2(a, 0.f) & 0xffffu);
}

template <int N>
__device__ __forceinline__ void waitv() {
  asm volatile("s_waitcnt vmcnt(%0)" ::"i"(N) : "memory");
}
template <int I>
struct IC {
  static constexpr int value = I;
};
template <int B, int E, class F>
__device__ __forceinline__ void static_for(F&& f) {
  if constexpr (B < E) {
    f(IC<B>{});
    static_for<B + 1, E>(f);
  }
}
#define FENCE() asm volatile("" ::: "memory")
#define BARRIER()                        \
  do {                                   \
    FENCE();                             \
    __builtin_amdgcn_s_barrier();        \
    FENCE();                             \
  } while (0)

template <int BM, int BN, int WM, int WN, int NS, bool BKN, bool ATR>
struct GC {
  static constexpr int NW = WM * WN;
  static constexpr int WTM = BM / WM, MT = WTM / 16;
  static constexpr int NTT = BN / 16;
  static constexpr int NT = (NTT + WN - 1) / WN;
  static constexpr int WTN = NT * 16;
  static constexpr int A_BYTES = BM * 128, B_BYTES = BN * 128;
  static constexpr int NSA = ATR ? 3 : NS;
  static constexpr int B_OFF = NSA * A_BYTES;
  static constexpr int TAB_OFF = B_OFF + NS * B_BYTES;
  static constexpr int NA = BM / 8, NBI = BN / 8;
  static constexpr int LA = ATR ? 0 : NA / 4;
  static constexpr int LB = (NBI + 3) / 4;
  static constexpr int NFULL = NBI % 4 == 0 ? 4 : NBI % 4;
  static constexpr int L = LA + LB;
  static constexpr int LDS = TAB_OFF + (ATR ? 8192 : 0);
  static_assert(NW == 8, "8 waves");
  static_assert(NA % 4 == 0, "A instr split");
  static_assert(NS >= 4, "stages");
  static_assert(LDS <= 163840, "lds");
};

template <class C, int BN, int WM, int WN, bool BKN>
struct GemmCtx {
  static constexpr int MT = C::MT, NT = C::NT, L = C::L, LA = C::LA, LB = C::LB;
  const char* Abase;
  const char* Bbase;
  size_t bstep;
  char* smem;
  unsigned goff[L > 0 ? L : 1];
  unsigned aoff[2], boff[2];
  int wq, par, last_n;
  bool full;

  __device__ __forceinline__ void init(const u16* Ab, int lda, const u16* Bb, int ldb, char* sm) {
    const int tid = threadIdx.x, lane = tid & 63;
    const int wave = __builtin_amdgcn_readfirstlane(tid >> 6);
    const int wm = wave % WM, wn = wave / WM;
    wq = wave & 3;
    par = wave >> 2;
    full = (C::NFULL == 4) || (wq < C::NFULL);
    smem = sm;
#pragma unroll
    for (int i = 0; i < LA; ++i) {
      int r = i * 4 + wq;
      int row = r * 8 + (lane >> 3);
      int lc = (lane & 7) ^ (row & 7);
      goff[i] = (unsigned)(row * lda + lc * 8) * 2u;
    }
#pragma unroll
    for (int i = 0; i < LB; ++i) {
      int r = i * 4 + wq;
      if (r >= C::NBI) r = 0;
      if (BKN) {
        int st = r * 4 + (lane >> 4);
        int kb = st / (BN / 16), nb = st % (BN / 16);
        int i16 = lane & 15, prow = i16 >> 1, hf = i16 & 1;
        int lrow = prow ^ ((kb & 1) << 2);
        goff[LA + i] = (unsigned)((kb * 8 + lrow) * ldb + nb * 16 + hf * 8) * 2u;
      } else {
        int row = r * 8 + (lane >> 3);
        int lc = (lane & 7) ^ (row & 7);
        goff[LA + i] = (unsigned)(row * ldb + lc * 8) * 2u;
      }
    }
    Abase = (const char*)Ab;
    Bbase = (const char*)Bb;
    bstep = BKN ? (size_t)64 * ldb * 2 : (size_t)128;
    const int g = lane >> 4, i16 = lane & 15;
#pragma unroll
    for (int s = 0; s < 2; ++s)
      aoff[s] = (unsigned)((wm * C::WTM + i16) * 128 + (((s * 4 + g) ^ (lane & 7)) << 4));
    if (BKN) {
      const int q4 = i16 >> 2, p = i16 & 3;
#pragma unroll
      for (int h = 0; h < 2; ++h)
        boff[h] = (unsigned)(C::B_OFF + (g * (BN / 16) + wn * NT) * 256 + (((h * 4 + q4) ^ ((g & 1) << 2)) * 32) +
                             p * 8);
    } else {
#pragma unroll
      for (int s = 0; s < 2; ++s)
        boff[s] = (unsigned)(C::B_OFF + (wn * C::WTN + i16) * 128 + (((s * 4 + g) ^ (lane & 7)) << 4));
    }
    const bool last_dummy = (C::NTT % WN != 0) && (wn * NT + NT > C::NTT);
    last_n = last_dummy ? 0 : NT - 1;
  }
  template <int i>
  __device__ __forceinline__ void issue1(int t, int sl) {
    if constexpr (i < LA) {
      __builtin_amdgcn_global_load_lds((const GAS unsigned*)(Abase + (size_t)t * 128 + goff[i]),
                                       (LAS unsigned*)(smem + sl * C::A_BYTES + wq * 1024 + i * 4096), 16, 0, 0);
    } else {
      if (i + 1 < L || full)
        __builtin_amdgcn_global_load_lds(
            (const GAS unsigned*)(Bbase + (size_t)t * bstep + goff[i]),
            (LAS unsigned*)(smem + C::B_OFF + sl * C::B_BYTES + wq * 1024 + (i - LA) * 4096), 16, 0, 0);
    }
  }
  __device__ __forceinline__ void issue_all(int t, int sl) {
    static_for<0, L>([&](auto II) { issue1<II.value>(t, sl); });
  }
  template <int n, int x>
  __device__ __forceinline__ void wait_st() {
    if (C::NFULL == 4 || n == 0) {
      waitv<n * L + x>();
    } else {
      if (full) waitv<n * L + x>();
      else waitv<n * (L - 1) + x>();
    }
  }
  template <int s>
  __device__ __forceinline__ void rd_frag(int sla, int slb, bf16x8 (&fa)[MT], bf16x8 (&fb)[NT]) {
    const char* sa = smem + sla * C::A_BYTES;
    const char* sb = smem + slb * C::B_BYTES;
#pragma unroll
    for (int m = 0; m < MT; ++m) fa[m] = *(const bf16x8*)(sa + aoff[s] + m * 2048);
#pragma unroll
    for (int n = 0; n < NT; ++n) {
      const int nn = (n == NT - 1) ? last_n : n;
      if (BKN) {
        bf16x4 lo = __builtin_amdgcn_ds_read_tr16_b64_v4bf16(
            (LAS bf16x4*)(sb + boff[0] + s * 4 * (BN / 16) * 256 + nn * 256));
        bf16x4 hi = __builtin_amdgcn_ds_read_tr16_b64_v4bf16(
            (LAS bf16x4*)(sb + boff[1] + s * 4 * (BN / 16) * 256 + nn * 256));
        fb[n] = __builtin_shufflevector(lo, hi, 0, 1, 2, 3, 4, 5, 6, 7);
      } else {
        fb[n] = *(const bf16x8*)(sb + boff[s] + nn * 2048);
      }
    }
  }
  template <bool ISS>
  __device__ __forceinline__ void mma(f32x4 (&acc)[MT][NT], bf16x8 (&fa)[MT], bf16x8 (&fb)[NT], int ti, int sl,
                                      bool do_issue) {
    static_for<0, MT * NT>([&](auto Q) {
      constexpr int q = Q.value, m = q / NT, n = q % NT;
      acc[m][n] = __builtin_amdgcn_mfma_f32_16x16x32_bf16(fb[n], fa[m], acc[m][n], 0, 0, 0);
      if constexpr (ISS) {
        constexpr int lo = (q * L) / (MT * NT), hi = ((q + 1) * L) / (MT * NT);
        if constexpr (hi > lo) {
          if (do_issue) static_for<lo, hi>([&](auto II) { issue1<II.value>(ti, sl); });
          __builtin_amdgcn_sched_barrier(0);
        }
      }
    });
  }
};

template <int BM, int BN, int WM, int WN, int NS, bool BKN, int T, int KID, class Pre, class Hook>
__device__ __forceinline__ void gemm_main(const u16* __restrict__ Ab, int lda, const u16* __restrict__ Bb, int ldb,
                                          char* smem,
                                          f32x4 (&acc)[GC<BM, BN, WM, WN, NS, BKN, false>::MT][GC<BM, BN, WM, WN, NS, BKN, false>::NT],
                                          Pre pre, Hook hook) {
  using C = GC<BM, BN, WM, WN, NS, BKN, false>;
  constexpr int MT = C::MT, NT = C::NT;
  GemmCtx<C, BN, WM, WN, BKN> g;
  g.init(Ab, lda, Bb, ldb, smem);
  static_for<0, NS - 1>([&](auto S) {
    if ((S.value & 1) == g.par) g.issue_all(S.value, S.value);
  });
  pre();
  if (g.par == 0) g.template wait_st<(NS - 2) / 2, 0>();
  BARRIER();
  bf16x8 fa0[MT], fb0[NT], fa1[MT], fb1[NT];
  g.template rd_frag<0>(0, 0, fa0, fb0);
  constexpr int U = (NS % 2 == 0) ? NS : 2 * NS;
  constexpr int NSTEADY = T - NS + 1;
  constexpr int NBLK = NSTEADY / U;
#pragma unroll 1
  for (int tb = 0; tb < NBLK * U; tb += U) {
    static_for<0, U>([&](auto UU) {
      constexpr int u = UU.value;
      const int t = tb + u;
      g.template rd_frag<1>(u % NS, u % NS, fa1, fb1);
      __builtin_amdgcn_sched_barrier(0);
      g.template mma<false>(acc, fa0, fb0, 0, 0, false);
      __builtin_amdgcn_sched_barrier(0);
      if (g.par == ((u + 1) & 1)) g.template wait_st<(NS - 3) / 2, 0>();
      BARRIER();
      g.template rd_frag<0>((u + 1) % NS, (u + 1) % NS, fa0, fb0);
      __builtin_amdgcn_sched_barrier(0);
      g.template mma<true>(acc, fa1, fb1, t + NS - 1, (u + NS - 1) % NS, g.par == ((u + NS - 1) & 1));
      hook(t);
      __builtin_amdgcn_sched_barrier(0);
    });
  }
  static_for<NBLK * U, NSTEADY>([&](auto TT) {
    constexpr int t = TT.value;
    g.template rd_frag<1>(t % NS, t % NS, fa1, fb1);
    __builtin_amdgcn_sched_barrier(0);
    g.template mma<false>(acc, fa0, fb0, 0, 0, false);
    __builtin_amdgcn_sched_barrier(0);
    if (g.par == ((t + 1) & 1)) g.template wait_st<(NS - 3) / 2, 0>();
    BARRIER();
    g.template rd_frag<0>((t + 1) % NS, (t + 1) % NS, fa0, fb0);
    __builtin_amdgcn_sched_barrier(0);
    g.template mma<true>(acc, fa1, fb1, t + NS - 1, (t + NS - 1) % NS, g.par == ((t + NS - 1) & 1));
    hook(t);
    __builtin_amdgcn_sched_barrier(0);
  });
  static_for<T - NS + 1, T>([&](auto TT) {
    constexpr int t = TT.value;
    g.template rd_frag<1>(t % NS, t % NS, fa1, fb1);
    __builtin_amdgcn_sched_barrier(0);
    g.template mma<false>(acc, fa0, fb0, 0, 0, false);
    __builtin_amdgcn_sched_barrier(0);
    if constexpr (t + 1 < T) {
      constexpr int younger = (T - 1) >= t + 3 ? (T - 1 - (t + 1)) / 2 : 0;
      if (g.par == ((t + 1) & 1)) g.template wait_st<younger, 0>();
      BARRIER();
      g.template rd_frag<0>((t + 1) % NS, (t + 1) % NS, fa0, fb0);
      __builtin_amdgcn_sched_barrier(0);
    }
    g.template mma<false>(acc, fa1, fb1, 0, 0, false);
    hook(t);
    __builtin_amdgcn_sched_barrier(0);
  });
}

__device__ __forceinline__ void store_pair(u16* rowp, u32x2 a, u32x2 b) {
  auto rx = __builtin_amdgcn_permlane16_swap(a[0], b[0], false, false);
  auto ry = __builtin_amdgcn_permlane16_swap(a[1], b[1], false, false);
  const int g = (threadIdx.x & 63) >> 4;
  u32x4 v = {rx[0], ry[0], rx[1], ry[1]};
  *(u32x4*)(rowp + (g & 1) * 16 + (g >> 1) * 8) = v;
}

struct NoHook {
  __device__ __forceinline__ void operator()(int) const {}
  __device__ __forceinline__ void operator()() const {}
};

__device__ __forceinline__ f32x2 ln_params(float mean, float var) {
  const float mb = rbf(mean), vb = rbf(var);
  const float sd = rbf(__fsqrt_rn(rbf(vb + 1e-5f)));
  const float ra = 1.f / sd;
  return f32x2{ra, -mb * ra};
}
__global__ __launch_bounds__(512) void k_pre(const u16* __restrict__ c, const u16* __restrict__ wmod,
                                             float* __restrict__ gates, float* __restrict__ tab,
                                             const u16* __restrict__ pw0) {
  __shared__ float red[2][128][33];
  __shared__ float sil[2][DM];
  const int tid = threadIdx.x;
  if (blockIdx.x < 144) {
    const int cg = tid & 3, kq = tid >> 2;
    const int col0 = blockIdx.x * 32 + cg * 8;
    u32x4 w[6];
#pragma unroll
    for (int i = 0; i < 6; ++i) {
      int k = i * 128 + kq;
      w[i] = *(const u32x4*)(wmod + (size_t)k * (6 * DM) + col0);
    }
#pragma unroll
    for (int i = 0; i < 3; ++i) {
      int idx = i * 512 + tid;
      float cv = bf2f(c[idx]);
      (&sil[0][0])[idx] = rbf(cv / (1.f + __expf(-cv)));
    }
    __syncthreads();
    float a0[8], a1[8];
#pragma unroll
    for (int j = 0; j < 8; ++j) a0[j] = a1[j] = 0.f;
#pragma unroll
    for (int i = 0; i < 6; ++i) {
      int k = i * 128 + kq;
      float s0 = sil[0][k], s1 = sil[1][k];
#pragma unroll
      for (int j = 0; j < 4; ++j) {
        float wl = bflo(w[i][j]), wh = bfhi(w[i][j]);
        a0[2 * j] += s0 * wl; a0[2 * j + 1] += s0 * wh;
        a1[2 * j] += s1 * wl; a1[2 * j + 1] += s1 * wh;
      }
    }
#pragma unroll
    for (int j = 0; j < 8; ++j) {
      red[0][kq][cg * 8 + j] = a0[j];
      red[1][kq][cg * 8 + j] = a1[j];
    }
    __syncthreads();
    {
      const int o = tid >> 3, part = tid & 7;
      const int b = o >> 5, cl = o & 31;
      float s = 0.f;
#pragma unroll
      for (int q = 0; q < 16; ++q) s += red[b][part * 16 + q][cl];
      s += __shfl_xor(s, 1);
      s += __shfl_xor(s, 2);
      s += __shfl_xor(s, 4);
      if (part == 0) {
        const float v = rbf(s);
        const int col = blockIdx.x * 32 + cl, i = col / DM, cc = col % DM;
        const int w_ = i / 3, kind = i % 3;
        if (kind == 2) gates[(b * 2 + w_) * DM + cc] = v;
        else if (kind == 1) tab[((b * 2 + w_) * 2 + 0) * DM + cc] = rbf(1.f + v);
        else tab[((b * 2 + w_) * 2 + 1) * DM + cc] = v;
      }
    }
  } else {
    const int wb = blockIdx.x - 144;
    constexpr int N0 = DM * 3 * DM / 64;
    unsigned vv[4];
#pragma unroll
    for (int q = 0; q < 4; ++q) {
      int l = (q * 14 + wb) * 512 + tid;
      l = l < N0 ? l : 0;
      vv[q] = *(const unsigned*)(pw0 + (size_t)l * 64);
    }
    unsigned accu = vv[0] + vv[1] + vv[2] + vv[3];
    if (accu == 0x9e3779b9u && tid == 999) gates[0] = 0.f;
  }
}

__global__ __launch_bounds__(256) void k_lnmod(const u16* __restrict__ x, const float* __restrict__ tab, int which,
                                               u16* __restrict__ out, const u16* __restrict__ pw1,
                                               const u16* __restrict__ pw2, const u16* __restrict__ pw3) {
  if (blockIdx.x >= MROWS / 4) {
    constexpr int N1 = DM * DM / 64, N2 = N1 + DM * FF / 64, N3 = N2 + FF * DM / 64;
    const int wb = blockIdx.x - MROWS / 4;
    unsigned vv[4];
#pragma unroll
    for (int q = 0; q < 4; ++q) {
      int l = (q * 81 + wb) * 256 + threadIdx.x;
      l = l < N3 ? l : 0;
      const u16* p = l < N1 ? pw1 + (size_t)l * 64 : (l < N2 ? pw2 + (size_t)(l - N1) * 64 : pw3 + (size_t)(l - N2) * 64);
      vv[q] = *(const unsigned*)p;
    }
    if (vv[0] + vv[1] + vv[2] + vv[3] == 0x9e3779b9u && threadIdx.x == 999) out[0] = 0;
    return;
  }
  const int lane = threadIdx.x & 63, row = blockIdx.x * 4 + (threadIdx.x >> 6);
  const int b = row >> 10;
  const u16* xr = x + (size_t)row * DM;
  float v[12];
#pragma unroll
  for (int i = 0; i < 3; ++i) {
    u32x2 u = *(const u32x2*)(xr + i * 256 + lane * 4);
    v[i * 4 + 0] = bflo(u[0]); v[i * 4 + 1] = bfhi(u[0]);
    v[i * 4 + 2] = bflo(u[1]); v[i * 4 + 3] = bfhi(u[1]);
  }
  const float* S = tab + (size_t)(b * 2 + which) * 2 * DM;
  const float* Tt = S + DM;
  f32x4 sc[3], sh[3];
#pragma unroll
  for (int i = 0; i < 3; ++i) {
    sc[i] = *(const f32x4*)(S + i * 256 + lane * 4);
    sh[i] = *(const f32x4*)(Tt + i * 256 + lane * 4);
  }
  float s = 0.f;
#pragma unroll
  for (int i = 0; i < 12; ++i) s += v[i];
#pragma unroll
  for (int o = 32; o; o >>= 1) s += __shfl_xor(s, o);
  const float mean = s * (1.f / DM);
  float q = 0.f;
#pragma unroll
  for (int i = 0; i < 12; ++i) { float d = v[i] - mean; q += d * d; }
#pragma unroll
  for (int o = 32; o; o >>= 1) q += __shfl_xor(q, o);
  const float mb = rbf(mean), vb = rbf(q * (1.f / DM));
  const float sd = rbf(__fsqrt_rn(rbf(vb + 1e-5f)));
#pragma unroll
  for (int i = 0; i < 3; ++i) {
    float h[4];
#pragma unroll
    for (int j = 0; j < 4; ++j) {
      float n = rbf(rbf(v[i * 4 + j] - mb) / sd);
      h[j] = rbf(n * sc[i][j]) + sh[i][j];
    }
    u32x2 o = {pack2(h[0], h[1]), pack2(h[2], h[3])};
    *(u32x2*)(out + (size_t)row * DM + i * 256 + lane * 4) = o;
  }
}

using GQKV = GC<128, 144, 4, 2, 4, true, false>;
__global__ __launch_bounds__(512, 2) void k_qkv(const u16* __restrict__ h, const u16* __restrict__ w,
                                                u16* __restrict__ qkv) {
  extern __shared__ __attribute__((aligned(16))) char smem[];
  using C = GQKV;
  constexpr int BM = 128, BN = 144;
  const int xc = blockIdx.x & 7, j = blockIdx.x >> 3;
  const int tm = (xc >> 2) * 8 + (j >> 2), tn = (xc & 3) * 4 + (j & 3);
  const int m0 = tm * BM, n0 = tn * BN;
  f32x4 acc[C::MT][C::NT] = {};
  gemm_main<BM, BN, 4, 2, 4, true, 12, 0>(h + (size_t)m0 * DM, DM, w + n0, 3 * DM, smem, acc, NoHook(), NoHook());
  const int lane = threadIdx.x & 63, wave = threadIdx.x >> 6, wm = wave % 4, wn = wave / 4;
#pragma unroll
  for (int m = 0; m < C::MT; ++m) {
    const int row = m0 + wm * C::WTM + m * 16 + (lane & 15);
    u16* rp = qkv + (size_t)row * (3 * DM) + n0 + wn * C::WTN;
    u32x2 v[C::NT];
#pragma unroll
    for (int n = 0; n < C::NT; ++n)
      v[n] = u32x2{pack2(acc[m][n][0], acc[m][n][1]), pack2(acc[m][n][2], acc[m][n][3])};
    store_pair(rp, v[0], v[1]);
    store_pair(rp + 32, v[2], v[3]);
    if (wn == 0) *(u32x2*)(rp + 64 + (lane >> 4) * 4) = v[4];
  }
}

using GSC = GC<128, 128, 4, 2, 4, false, false>;
__global__ __launch_bounds__(512, 2) void k_scores(const u16* __restrict__ qkv, u16* __restrict__ E,
                                                   f32x2* __restrict__ st) {
  extern __shared__ __attribute__((aligned(16))) char smem[];
  using C = GSC;
  constexpr int BM = 128, BN = 128;
  const int xc = blockIdx.x & 7, j = blockIdx.x >> 3;
  const int bh = xc >> 1, b = bh >> 1, hh = bh & 1;
  const int tm = (xc & 1) * 4 + (j >> 3), tn = j & 7;
  const int m0 = tm * BM, n0 = tn * BN;
  const u16* Q = qkv + (size_t)(b * NSEQ + m0) * (3 * DM) + hh * DH;
  const u16* K = qkv + (size_t)(b * NSEQ + n0) * (3 * DM) + DM + hh * DH;
  f32x4 acc[C::MT][C::NT] = {};
  gemm_main<BM, BN, 4, 2, 4, false, 6, 1>(Q, 3 * DM, K, 3 * DM, smem, acc, NoHook(), NoHook());
  const int lane = threadIdx.x & 63, wave = threadIdx.x >> 6, wm = wave % 4, wn = wave / 4;
  const float scale = 0.05103103630798288f;
#pragma unroll
  for (int m = 0; m < C::MT; ++m) {
    float s[C::NT][4];
    float mx = -INFINITY;
#pragma unroll
    for (int n = 0; n < C::NT; ++n) {
      unsigned p0 = pack2(acc[m][n][0], acc[m][n][1]), p1 = pack2(acc[m][n][2], acc[m][n][3]);
      p0 = pack2(bflo(p0) * scale, bfhi(p0) * scale);
      p1 = pack2(bflo(p1) * scale, bfhi(p1) * scale);
      s[n][0] = bflo(p0); s[n][1] = bfhi(p0); s[n][2] = bflo(p1); s[n][3] = bfhi(p1);
      mx = fmaxf(mx, fmaxf(fmaxf(s[n][0], s[n][1]), fmaxf(s[n][2], s[n][3])));
    }
    mx = fmaxf(mx, __shfl_xor(mx, 16));
    mx = fmaxf(mx, __shfl_xor(mx, 32));
    float sum = 0.f;
    const int row = m0 + wm * C::WTM + m * 16 + (lane & 15);
    u16* Er = E + ((size_t)bh * NSEQ + row) * NSEQ + n0 + wn * C::WTN;
    u32x2 v[C::NT];
#pragma unroll
    for (int n = 0; n < C::NT; ++n) {
#pragma unroll
      for (int e = 0; e < 4; ++e) {
        s[n][e] = __expf(s[n][e] - mx);
        sum += s[n][e];
      }
      v[n] = u32x2{pack2(s[n][0], s[n][1]), pack2(s[n][2], s[n][3])};
    }
    store_pair(Er, v[0], v[1]);
    store_pair(Er + 32, v[2], v[3]);
    sum += __shfl_xor(sum, 16);
    sum += __shfl_xor(sum, 32);
    if (lane < 16) st[((size_t)bh * NSEQ + row) * 16 + tn * 2 + wn] = f32x2{mx, sum};
  }
}

using GPV = GC<64, 96, 4, 2, 6, true, false>;
__global__ __launch_bounds__(512, 2) void k_pv(const u16* __restrict__ E, const f32x2* __restrict__ st,
                                               const u16* __restrict__ qkv, u16* __restrict__ O) {
  extern __shared__ __attribute__((aligned(16))) char smem[];
  using C = GPV;
  constexpr int BM = 64, BN = 96;
  const int xc = blockIdx.x & 7, j = blockIdx.x >> 3;
  const int bh = xc >> 1, b = bh >> 1, hh = bh & 1;
  const int tm = (xc & 1) * 8 + (j >> 2), tn = j & 3;
  const int m0 = tm * BM, n0 = tn * BN;
  const int lane = threadIdx.x & 63, wave = threadIdx.x >> 6, wm = wave % 4, wn = wave / 4;
  float* ftab = (float*)(smem + C::LDS);
  auto pre = [&]() {
    if (threadIdx.x < 64) {
      const f32x4* p = (const f32x4*)(st + ((size_t)bh * NSEQ + m0 + threadIdx.x) * 16);
      f32x4 v[8];
#pragma unroll
      for (int t = 0; t < 8; ++t) v[t] = p[t];
      float M = -INFINITY;
#pragma unroll
      for (int t = 0; t < 8; ++t) M = fmaxf(M, fmaxf(v[t][0], v[t][2]));
      float fac[16], Ls = 0.f;
#pragma unroll
      for (int t = 0; t < 8; ++t) {
        fac[2 * t] = __expf(v[t][0] - M);
        fac[2 * t + 1] = __expf(v[t][2] - M);
        Ls += fac[2 * t] * v[t][1] + fac[2 * t + 1] * v[t][3];
      }
      float inv = 1.f / Ls;
#pragma unroll
      for (int t = 0; t < 16; ++t) ftab[t * 64 + threadIdx.x] = fac[t] * inv;
      asm volatile("s_waitcnt lgkmcnt(0)" ::: "memory");
    }
  };
  const u16* A = E + ((size_t)bh * NSEQ + m0) * NSEQ;
  const u16* V = qkv + (size_t)(b * NSEQ) * (3 * DM) + 2 * DM + hh * DH + n0;
  f32x4 acc[1][3] = {};
  f32x4 tot[1][3] = {};
  const float* frow = ftab + wm * 16 + (lane & 15);
  auto hook = [&](int t) {
    const float f = frow[t * 64];
#pragma unroll
    for (int n = 0; n < 3; ++n) {
      tot[0][n] += acc[0][n] * f;
      acc[0][n] = f32x4{0.f, 0.f, 0.f, 0.f};
    }
  };
  gemm_main<BM, BN, 4, 2, 6, true, 16, 2>(A, NSEQ, V, 3 * DM, smem, acc, pre, hook);
#pragma unroll
  for (int n = 0; n < 3; ++n) {
    int row = b * NSEQ + m0 + wm * 16 + (lane & 15);
    int col = hh * DH + n0 + wn * C::WTN + n * 16 + (lane >> 4) * 4;
    u32x2 v = {pack2(tot[0][n][0], tot[0][n][1]), pack2(tot[0][n][2], tot[0][n][3])};
    *(u32x2*)(O + (size_t)row * DM + col) = v;
  }
}

template <int KI, int LDA, bool STATS>
__device__ __forceinline__ void res_gemm(const u16* __restrict__ A, const u16* __restrict__ W,
                                         const u16* __restrict__ xin, const float* __restrict__ gates,
                                         u16* __restrict__ xo, f32x2* __restrict__ pst, char* smem) {
  using C = GPV;
  constexpr int BM = 64, BN = 96;
  const int xc = blockIdx.x & 7, j = blockIdx.x >> 3;
  const int tm = (xc >> 1) * 8 + (j >> 2), tn = (xc & 1) * 4 + (j & 3);
  const int m0 = tm * BM, n0 = tn * BN, b = m0 >> 10;
  const int lane = threadIdx.x & 63, wave = threadIdx.x >> 6, wm = wave % 4, wn = wave / 4;
  const int row = m0 + wm * 16 + (lane & 15);
  const int col0 = n0 + wn * C::WTN + (lane >> 4) * 4;
  f32x4 acc[1][3] = {};
  gemm_main<BM, BN, 4, 2, 6, true, LDA / 64, KI>(A + (size_t)m0 * LDA, LDA, W + n0, DM, smem, acc, NoHook(), NoHook());
  float vals[12];
#pragma unroll
  for (int n = 0; n < 3; ++n) {
    u32x2 xv = *(const u32x2*)(xin + (size_t)row * DM + col0 + n * 16);
    f32x4 gt = *(const f32x4*)(gates + b * (2 * DM) + col0 + n * 16);
    unsigned a0 = pack2(acc[0][n][0], acc[0][n][1]), a1 = pack2(acc[0][n][2], acc[0][n][3]);
    unsigned g0 = pack2(gt[0] * bflo(a0), gt[1] * bfhi(a0));
    unsigned g1 = pack2(gt[2] * bflo(a1), gt[3] * bfhi(a1));
    u32x2 o = {pack2(bflo(xv[0]) + bflo(g0), bfhi(xv[0]) + bfhi(g0)),
               pack2(bflo(xv[1]) + bflo(g1), bfhi(xv[1]) + bfhi(g1))};
    *(u32x2*)(xo + (size_t)row * DM + col0 + n * 16) = o;
    vals[n * 4 + 0] = bflo(o[0]); vals[n * 4 + 1] = bfhi(o[0]);
    vals[n * 4 + 2] = bflo(o[1]); vals[n * 4 + 3] = bfhi(o[1]);
  }
  if (STATS) {
    float s = 0.f;
#pragma unroll
    for (int i = 0; i < 12; ++i) s += vals[i];
    s += __shfl_xor(s, 16);
    s += __shfl_xor(s, 32);
    const float lm = s * (1.f / 48.f);
    float q = 0.f;
#pragma unroll
    for (int i = 0; i < 12; ++i) { float d = vals[i] - lm; q += d * d; }
    q += __shfl_xor(q, 16);
    q += __shfl_xor(q, 32);
    if (lane < 16) pst[(size_t)row * 16 + tn * 2 + wn] = f32x2{s, q};
  }
}
__global__ __launch_bounds__(512, 2) void k_wo(const u16* __restrict__ O, const u16* __restrict__ wo,
                                               const u16* __restrict__ xin, const float* __restrict__ gates,
                                               u16* __restrict__ x1, f32x2* __restrict__ pst) {
  extern __shared__ __attribute__((aligned(16))) char smem[];
  res_gemm<3, DM, false>(O, wo, xin, gates, x1, pst, smem);
}
__global__ __launch_bounds__(512, 2) void k_fc2(const u16* __restrict__ f, const u16* __restrict__ w2,
                                                const u16* __restrict__ x1, const float* __restrict__ gates,
                                                u16* __restrict__ out) {
  extern __shared__ __attribute__((aligned(16))) char smem[];
  res_gemm<5, FF, false>(f, w2, x1, gates + DM, out, nullptr, smem);
}

__device__ __forceinline__ float gelu_tanh(float u) {
  float w = u * (-2.f * 0.7978845608028654f * 1.4426950408889634f);
  float a = w * (1.f + 0.044715f * u * u);
  return u * __builtin_amdgcn_rcpf(1.f + __builtin_amdgcn_exp2f(a));
}
using GFC = GC<128, 192, 2, 4, 4, true, false>;
__global__ __launch_bounds__(512, 2) void k_fc1(const u16* __restrict__ h2, const u16* __restrict__ w1,
                                                u16* __restrict__ f) {
  extern __shared__ __attribute__((aligned(16))) char smem[];
  using C = GFC;
  constexpr int BM = 128, BN = 192;
  const int xc = blockIdx.x & 7, j = blockIdx.x >> 3;
  const int tm = (xc >> 2) * 8 + (j >> 2), tn = (xc & 3) * 4 + (j & 3);
  const int m0 = tm * BM, n0 = tn * BN;
  f32x4 acc[C::MT][C::NT] = {};
  gemm_main<BM, BN, 2, 4, 4, true, 12, 4>(h2 + (size_t)m0 * DM, DM, w1 + n0, FF, smem, acc, NoHook(), NoHook());
  const int lane = threadIdx.x & 63, wave = threadIdx.x >> 6, wm = wave % 2, wn = wave / 2;
#pragma unroll
  for (int m = 0; m < C::MT; ++m) {
    const int row = m0 + wm * C::WTM + m * 16 + (lane & 15);
    u16* rp = f + (size_t)row * FF + n0 + wn * C::WTN;
    u32x2 v[C::NT];
#pragma unroll
    for (int n = 0; n < C::NT; ++n) {
      unsigned p0 = pack2(acc[m][n][0], acc[m][n][1]), p1 = pack2(acc[m][n][2], acc[m][n][3]);
      v[n] = u32x2{pack2(gelu_tanh(bflo(p0)), gelu_tanh(bfhi(p0))), pack2(gelu_tanh(bflo(p1)), gelu_tanh(bfhi(p1)))};
    }
    store_pair(rp, v[0], v[1]);
    *(u32x2*)(rp + 32 + (lane >> 4) * 4) = v[2];
  }
}

extern "C" void kernel_launch(void* const* d_in, const int* in_sizes, int n_in, void* d_out, int out_size,
                              void* d_ws, size_t ws_size, hipStream_t stream) {
  const u16* x = (const u16*)d_in[0];
  const u16* c = (const u16*)d_in[1];
  const u16* wqkv = (const u16*)d_in[2];
  const u16* wo = (const u16*)d_in[3];
  const u16* wmod = (const u16*)d_in[4];
  const u16* w1 = (const u16*)d_in[5];
  const u16* w2 = (const u16*)d_in[6];
  u16* out = (u16*)d_out;

  char* ws = (char*)d_ws;
  size_t o = 0;
  auto take = [&](size_t bytes) { char* p = ws + o; o += (bytes + 255) & ~(size_t)255; return p; };
  float* gates = (float*)take(2 * 2 * DM * 4);
  float* tab = (float*)take(2 * 2 * 2 * DM * 4);
  f32x2* st = (f32x2*)take((size_t)4 * NSEQ * 16 * 8);
  u16* h = (u16*)take((size_t)MROWS * DM * 2);
  u16* O = h;
  u16* h2 = h;
  u16* x1 = (u16*)take((size_t)MROWS * DM * 2);
  u16* qkv = (u16*)take((size_t)MROWS * 3 * DM * 2);
  u16* E = (u16*)take((size_t)4 * NSEQ * NSEQ * 2);
  u16* f = qkv;
  f32x2* pst = nullptr;

  constexpr int LDS_QKV = GQKV::LDS;
  constexpr int LDS_SC = GSC::LDS;
  constexpr int LDS_PV = GPV::LDS + 16 * 64 * 4;
  constexpr int LDS_WO = GPV::LDS;
  constexpr int LDS_FC = GFC::LDS;
  (void)hipFuncSetAttribute((const void*)k_qkv, hipFuncAttributeMaxDynamicSharedMemorySize, LDS_QKV);
  (void)hipFuncSetAttribute((const void*)k_scores, hipFuncAttributeMaxDynamicSharedMemorySize, LDS_SC);
  (void)hipFuncSetAttribute((const void*)k_pv, hipFuncAttributeMaxDynamicSharedMemorySize, LDS_PV);
  (void)hipFuncSetAttribute((const void*)k_wo, hipFuncAttributeMaxDynamicSharedMemorySize, LDS_WO);
  (void)hipFuncSetAttribute((const void*)k_fc1, hipFuncAttributeMaxDynamicSharedMemorySize, LDS_FC);
  (void)hipFuncSetAttribute((const void*)k_fc2, hipFuncAttributeMaxDynamicSharedMemorySize, LDS_WO);

  k_pre<<<144 + 14, 512, 0, stream>>>(c, wmod, gates, tab, wqkv);
  k_lnmod<<<MROWS / 4 + 81, 256, 0, stream>>>(x, tab, 0, h, wo, w1, w2);
  k_qkv<<<256, 512, LDS_QKV, stream>>>(h, wqkv, qkv);
  k_scores<<<256, 512, LDS_SC, stream>>>(qkv, E, st);
  k_pv<<<256, 512, LDS_PV, stream>>>(E, st, qkv, O);
  k_wo<<<256, 512, LDS_WO, stream>>>(O, wo, x, gates, x1, pst);
  k_lnmod<<<MROWS / 4, 256, 0, stream>>>(x1, tab, 1, h2, wo, w1, w2);
  k_fc1<<<256, 512, LDS_FC, stream>>>(h2, w1, f);
  k_fc2<<<256, 512, LDS_WO, stream>>>(f, w2, x1, gates, out);
}
```

```cpp
#include <hip/hip_runtime.h>
#include <hip/hip_bf16.h>
#include <stdint.h>

typedef unsigned short u16;
typedef __attribute__((ext_vector_type(8))) __bf16 bf16x8;
typedef __attribute__((ext_vector_type(4))) __bf16 bf16x4;
typedef __attribute__((ext_vector_type(2))) __bf16 bf16x2;
typedef __attribute__((ext_vector_type(4))) float f32x4;
typedef __attribute__((ext_vector_type(2))) float f32x2;
typedef __attribute__((ext_vector_type(4))) unsigned u32x4;
typedef __attribute__((ext_vector_type(2))) unsigned u32x2;
#define LAS __attribute__((address_space(3)))
#define GAS __attribute__((address_space(1)))

constexpr int NB = 2, NSEQ = 1024, DM = 768, NH = 2, DH = 384, FF = 3072;
constexpr int MROWS = NB * NSEQ;

__device__ __forceinline__ float bf2f(u16 v) { return __uint_as_float(((unsigned)v) << 16); }
__device__ __forceinline__ float bflo(unsigned v) { return __uint_as_float(v << 16); }
__device__ __forceinline__ float bfhi(unsigned v) { return __uint_as_float(v & 0xffff0000u); }
__device__ __forceinline__ unsigned pack2(float a, float b) {
  f32x2 f = {a, b};
  bf16x2 r = __builtin_convertvector(f, bf16x2);
  return __builtin_bit_cast(unsigned, r);
}
__device__ __forceinline__ float rbf(float a) {
  return bflo(pack2(a, 0.f) & 0xffffu);
}

template <int N>
__device__ __forceinline__ void waitv() {
  asm volatile("s_waitcnt vmcnt(%0)" ::"i"(N) : "memory");
}
template <int I>
struct IC {
  static constexpr int value = I;
};
template <int B, int E, class F>
__device__ __forceinline__ void static_for(F&& f) {
  if constexpr (B < E) {
    f(IC<B>{});
    static_for<B + 1, E>(f);
  }
}
#define FENCE() asm volatile("" ::: "memory")
#define BARRIER()                        \
  do {                                   \
    FENCE();                             \
    __builtin_amdgcn_s_barrier();        \
    FENCE();                             \
  } while (0)

template <int BM, int BN, int WM, int WN, int NS, bool BKN, bool ATR>
struct GC {
  static constexpr int NW = WM * WN;
  static constexpr int WTM = BM / WM, MT = WTM / 16;
  static constexpr int NTT = BN / 16;
  static constexpr int NT = (NTT + WN - 1) / WN;
  static constexpr int WTN = NT * 16;
  static constexpr int A_BYTES = BM * 128, B_BYTES = BN * 128;
  static constexpr int NSA = ATR ? 3 : NS;
  static constexpr int B_OFF = NSA * A_BYTES;
  static constexpr int TAB_OFF = B_OFF + NS * B_BYTES;
  static constexpr int NA = BM / 8, NBI = BN / 8;
  static constexpr int LA = ATR ? 0 : NA / 4;
  static constexpr int LB = (NBI + 3) / 4;
  static constexpr int NFULL = NBI % 4 == 0 ? 4 : NBI % 4;
  static constexpr int L = LA + LB;
  static constexpr int LDS = TAB_OFF + (ATR ? 8192 : 0);
  static_assert(NW == 8, "8 waves");
  static_assert(NA % 4 == 0, "A instr split");
  static_assert(NS >= 4, "stages");
  static_assert(LDS <= 163840, "lds");
};

template <class C, int BN, int WM, int WN, bool BKN>
struct GemmCtx {
  static constexpr int MT = C::MT, NT = C::NT, L = C::L, LA = C::LA, LB = C::LB;
  const char* Abase;
  const char* Bbase;
  size_t bstep;
  char* smem;
  unsigned goff[L > 0 ? L : 1];
  unsigned aoff[2], boff[2];
  int wq, par, last_n;
  bool full;

  __device__ __forceinline__ void init(const u16* Ab, int lda, const u16* Bb, int ldb, char* sm) {
    const int tid = threadIdx.x, lane = tid & 63;
    const int wave = __builtin_amdgcn_readfirstlane(tid >> 6);
    const int wm = wave % WM, wn = wave / WM;
    wq = wave & 3;
    par = wave >> 2;
    full = (C::NFULL == 4) || (wq < C::NFULL);
    smem = sm;
#pragma unroll
    for (int i = 0; i < LA; ++i) {
      int r = i * 4 + wq;
      int row = r * 8 + (lane >> 3);
      int lc = (lane & 7) ^ (row & 7);
      goff[i] = (unsigned)(row * lda + lc * 8) * 2u;
    }
#pragma unroll
    for (int i = 0; i < LB; ++i) {
      int r = i * 4 + wq;
      if (r >= C::NBI) r = 0;
      if (BKN) {
        int st = r * 4 + (lane >> 4);
        int kb = st / (BN / 16), nb = st % (BN / 16);
        int i16 = lane & 15, prow = i16 >> 1, hf = i16 & 1;
        int lrow = prow ^ ((kb & 1) << 2);
        goff[LA + i] = (unsigned)((kb * 8 + lrow) * ldb + nb * 16 + hf * 8) * 2u;
      } else {
        int row = r * 8 + (lane >> 3);
        int lc = (lane & 7) ^ (row & 7);
        goff[LA + i] = (unsigned)(row * ldb + lc * 8) * 2u;
      }
    }
    Abase = (const char*)Ab;
    Bbase = (const char*)Bb;
    bstep = BKN ? (size_t)64 * ldb * 2 : (size_t)128;
    const int g = lane >> 4, i16 = lane & 15;
#pragma unroll
    for (int s = 0; s < 2; ++s)
      aoff[s] = (unsigned)((wm * C::WTM + i16) * 128 + (((s * 4 + g) ^ (lane & 7)) << 4));
    if (BKN) {
      const int q4 = i16 >> 2, p = i16 & 3;
#pragma unroll
      for (int h = 0; h < 2; ++h)
        boff[h] = (unsigned)(C::B_OFF + (g * (BN / 16) + wn * NT) * 256 + (((h * 4 + q4) ^ ((g & 1) << 2)) * 32) +
                             p * 8);
    } else {
#pragma unroll
      for (int s = 0; s < 2; ++s)
        boff[s] = (unsigned)(C::B_OFF + (wn * C::WTN + i16) * 128 + (((s * 4 + g) ^ (lane & 7)) << 4));
    }
    const bool last_dummy = (C::NTT % WN != 0) && (wn * NT + NT > C::NTT);
    last_n = last_dummy ? 0 : NT - 1;
  }
  template <int i>
  __device__ __forceinline__ void issue1(int t, int sl) {
    if constexpr (i < LA) {
      __builtin_amdgcn_global_load_lds((const GAS unsigned*)(Abase + (size_t)t * 128 + goff[i]),
                                       (LAS unsigned*)(smem + sl * C::A_BYTES + wq * 1024 + i * 4096), 16, 0, 0);
    } else {
      if (i + 1 < L || full)
        __builtin_amdgcn_global_load_lds(
            (const GAS unsigned*)(Bbase + (size_t)t * bstep + goff[i]),
            (LAS unsigned*)(smem + C::B_OFF + sl * C::B_BYTES + wq * 1024 + (i - LA) * 4096), 16, 0, 0);
    }
  }
  __device__ __forceinline__ void issue_all(int t, int sl) {
    static_for<0, L>([&](auto II) { issue1<II.value>(t, sl); });
  }
  template <int n, int x>
  __device__ __forceinline__ void wait_st() {
    if (C::NFULL == 4 || n == 0) {
      waitv<n * L + x>();
    } else {
      if (full) waitv<n * L + x>();
      else waitv<n * (L - 1) + x>();
    }
  }
  template <int s>
  __device__ __forceinline__ void rd_frag(int sla, int slb, bf16x8 (&fa)[MT], bf16x8 (&fb)[NT]) {
    const char* sa = smem + sla * C::A_BYTES;
    const char* sb = smem + slb * C::B_BYTES;
#pragma unroll
    for (int m = 0; m < MT; ++m) fa[m] = *(const bf16x8*)(sa + aoff[s] + m * 2048);
#pragma unroll
    for (int n = 0; n < NT; ++n) {
      const int nn = (n == NT - 1) ? last_n : n;
      if (BKN) {
        bf16x4 lo = __builtin_amdgcn_ds_read_tr16_b64_v4bf16(
            (LAS bf16x4*)(sb + boff[0] + s * 4 * (BN / 16) * 256 + nn * 256));
        bf16x4 hi = __builtin_amdgcn_ds_read_tr16_b64_v4bf16(
            (LAS bf16x4*)(sb + boff[1] + s * 4 * (BN / 16) * 256 + nn * 256));
        fb[n] = __builtin_shufflevector(lo, hi, 0, 1, 2, 3, 4, 5, 6, 7);
      } else {
        fb[n] = *(const bf16x8*)(sb + boff[s] + nn * 2048);
      }
    }
  }
  template <bool ISS>
  __device__ __forceinline__ void mma(f32x4 (&acc)[MT][NT], bf16x8 (&fa)[MT], bf16x8 (&fb)[NT], int ti, int sl,
                                      bool do_issue) {
    static_for<0, MT * NT>([&](auto Q) {
      constexpr int q = Q.value, m = q / NT, n = q % NT;
      acc[m][n] = __builtin_amdgcn_mfma_f32_16x16x32_bf16(fb[n], fa[m], acc[m][n], 0, 0, 0);
      if constexpr (ISS) {
        constexpr int lo = (q * L) / (MT * NT), hi = ((q + 1) * L) / (MT * NT);
        if constexpr (hi > lo) {
          if (do_issue) static_for<lo, hi>([&](auto II) { issue1<II.value>(ti, sl); });
          __builtin_amdgcn_sched_barrier(0);
        }
      }
    });
  }
};

template <int BM, int BN, int WM, int WN, int NS, bool BKN, int T, int KID, class Pre, class Hook>
__device__ __forceinline__ void gemm_main(const u16* __restrict__ Ab, int lda, const u16* __restrict__ Bb, int ldb,
                                          char* smem,
                                          f32x4 (&acc)[GC<BM, BN, WM, WN, NS, BKN, false>::MT][GC<BM, BN, WM, WN, NS, BKN, false>::NT],
                                          Pre pre, Hook hook) {
  using C = GC<BM, BN, WM, WN, NS, BKN, false>;
  constexpr int MT = C::MT, NT = C::NT;
  GemmCtx<C, BN, WM, WN, BKN> g;
  g.init(Ab, lda, Bb, ldb, smem);
  static_for<0, NS - 1>([&](auto S) {
    if ((S.value & 1) == g.par) g.issue_all(S.value, S.value);
  });
  pre();
  if (g.par == 0) g.template wait_st<(NS - 2) / 2, 0>();
  BARRIER();
  bf16x8 fa0[MT], fb0[NT], fa1[MT], fb1[NT];
  g.template rd_frag<0>(0, 0, fa0, fb0);
  constexpr int U = (NS % 2 == 0) ? NS : 2 * NS;
  constexpr int NSTEADY = T - NS + 1;
  constexpr int NBLK = NSTEADY / U;
#pragma unroll 1
  for (int tb = 0; tb < NBLK * U; tb += U) {
    static_for<0, U>([&](auto UU) {
      constexpr int u = UU.value;
      const int t = tb + u;
      g.template rd_frag<1>(u % NS, u % NS, fa1, fb1);
      __builtin_amdgcn_sched_barrier(0);
      g.template mma<false>(acc, fa0, fb0, 0, 0, false);
      __builtin_amdgcn_sched_barrier(0);
      if (g.par == ((u + 1) & 1)) g.template wait_st<(NS - 3) / 2, 0>();
      BARRIER();
      g.template rd_frag<0>((u + 1) % NS, (u + 1) % NS, fa0, fb0);
      __builtin_amdgcn_sched_barrier(0);
      g.template mma<true>(acc, fa1, fb1, t + NS - 1, (u + NS - 1) % NS, g.par == ((u + NS - 1) & 1));
      hook(t);
      __builtin_amdgcn_sched_barrier(0);
    });
  }
  static_for<NBLK * U, NSTEADY>([&](auto TT) {
    constexpr int t = TT.value;
    g.template rd_frag<1>(t % NS, t % NS, fa1, fb1);
    __builtin_amdgcn_sched_barrier(0);
    g.template mma<false>(acc, fa0, fb0, 0, 0, false);
    __builtin_amdgcn_sched_barrier(0);
    if (g.par == ((t + 1) & 1)) g.template wait_st<(NS - 3) / 2, 0>();
    BARRIER();
    g.template rd_frag<0>((t + 1) % NS, (t + 1) % NS, fa0, fb0);
    __builtin_amdgcn_sched_barrier(0);
    g.template mma<true>(acc, fa1, fb1, t + NS - 1, (t + NS - 1) % NS, g.par == ((t + NS - 1) & 1));
    hook(t);
    __builtin_amdgcn_sched_barrier(0);
  });
  static_for<T - NS + 1, T>([&](auto TT) {
    constexpr int t = TT.value;
    g.template rd_frag<1>(t % NS, t % NS, fa1, fb1);
    __builtin_amdgcn_sched_barrier(0);
    g.template mma<false>(acc, fa0, fb0, 0, 0, false);
    __builtin_amdgcn_sched_barrier(0);
    if constexpr (t + 1 < T) {
      constexpr int younger = (T - 1) >= t + 3 ? (T - 1 - (t + 1)) / 2 : 0;
      if (g.par == ((t + 1) & 1)) g.template wait_st<younger, 0>();
      BARRIER();
      g.template rd_frag<0>((t + 1) % NS, (t + 1) % NS, fa0, fb0);
      __builtin_amdgcn_sched_barrier(0);
    }
    g.template mma<false>(acc, fa1, fb1, 0, 0, false);
    hook(t);
    __builtin_amdgcn_sched_barrier(0);
  });
}

__device__ __forceinline__ void store_pair(u16* rowp, u32x2 a, u32x2 b) {
  auto rx = __builtin_amdgcn_permlane16_swap(a[0], b[0], false, false);
  auto ry = __builtin_amdgcn_permlane16_swap(a[1], b[1], false, false);
  const int g = (threadIdx.x & 63) >> 4;
  u32x4 v = {rx[0], ry[0], rx[1], ry[1]};
  *(u32x4*)(rowp + (g & 1) * 16 + (g >> 1) * 8) = v;
}

struct NoHook {
  __device__ __forceinline__ void operator()(int) const {}
  __device__ __forceinline__ void operator()() const {}
};

__device__ __forceinline__ f32x2 ln_params(float mean, float var) {
  const float mb = rbf(mean), vb = rbf(var);
  const float sd = rbf(__fsqrt_rn(rbf(vb + 1e-5f)));
  const float ra = 1.f / sd;
  return f32x2{ra, -mb * ra};
}
__global__ __launch_bounds__(512) void k_pre(const u16* __restrict__ c, const u16* __restrict__ wmod,
                                             float* __restrict__ gates, float* __restrict__ tab,
                                             const u16* __restrict__ pw0) {
  __shared__ float red[2][128][33];
  __shared__ float sil[2][DM];
  const int tid = threadIdx.x;
  if (blockIdx.x < 144) {
    const int cg = tid & 3, kq = tid >> 2;
    const int col0 = blockIdx.x * 32 + cg * 8;
    u32x4 w[6];
#pragma unroll
    for (int i = 0; i < 6; ++i) {
      int k = i * 128 + kq;
      w[i] = *(const u32x4*)(wmod + (size_t)k * (6 * DM) + col0);
    }
#pragma unroll
    for (int i = 0; i < 3; ++i) {
      int idx = i * 512 + tid;
      float cv = bf2f(c[idx]);
      (&sil[0][0])[idx] = rbf(cv / (1.f + __expf(-cv)));
    }
    __syncthreads();
    float a0[8], a1[8];
#pragma unroll
    for (int j = 0; j < 8; ++j) a0[j] = a1[j] = 0.f;
#pragma unroll
    for (int i = 0; i < 6; ++i) {
      int k = i * 128 + kq;
      float s0 = sil[0][k], s1 = sil[1][k];
#pragma unroll
      for (int j = 0; j < 4; ++j) {
        float wl = bflo(w[i][j]), wh = bfhi(w[i][j]);
        a0[2 * j] += s0 * wl; a0[2 * j + 1] += s0 * wh;
        a1[2 * j] += s1 * wl; a1[2 * j + 1] += s1 * wh;
      }
    }
#pragma unroll
    for (int j = 0; j < 8; ++j) {
      red[0][kq][cg * 8 + j] = a0[j];
      red[1][kq][cg * 8 + j] = a1[j];
    }
    __syncthreads();
    {
      const int o = tid >> 3, part = tid & 7;
      const int b = o >> 5, cl = o & 31;
      float s = 0.f;
#pragma unroll
      for (int q = 0; q < 16; ++q) s += red[b][part * 16 + q][cl];
      s += __shfl_xor(s, 1);
      s += __shfl_xor(s, 2);
      s += __shfl_xor(s, 4);
      if (part == 0) {
        const float v = rbf(s);
        const int col = blockIdx.x * 32 + cl, i = col / DM, cc = col % DM;
        const int w_ = i / 3, kind = i % 3;
        if (kind == 2) gates[(b * 2 + w_) * DM + cc] = v;
        else if (kind == 1) tab[((b * 2 + w_) * 2 + 0) * DM + cc] = rbf(1.f + v);
        else tab[((b * 2 + w_) * 2 + 1) * DM + cc] = v;
      }
    }
  } else {
    const int wb = blockIdx.x - 144;
    constexpr int N0 = DM * 3 * DM / 64;
    unsigned vv[4];
#pragma unroll
    for (int q = 0; q < 4; ++q) {
      int l = (q * 14 + wb) * 512 + tid;
      l = l < N0 ? l : 0;
      vv[q] = *(const unsigned*)(pw0 + (size_t)l * 64);
    }
    unsigned accu = vv[0] + vv[1] + vv[2] + vv[3];
    if (accu == 0x9e3779b9u && tid == 999) gates[0] = 0.f;
  }
}

__global__ __launch_bounds__(256) void k_lnmod(const u16* __restrict__ x, const float* __restrict__ tab, int which,
                                               u16* __restrict__ out, const u16* __restrict__ pw1,
                                               const u16* __restrict__ pw2, const u16* __restrict__ pw3) {
  if (blockIdx.x >= MROWS / 4) {
    constexpr int N1 = DM * DM / 64, N2 = N1 + DM * FF / 64, N3 = N2 + FF * DM / 64;
    const int wb = blockIdx.x - MROWS / 4;
    unsigned vv[4];
#pragma unroll
    for (int q = 0; q < 4; ++q) {
      int l = (q * 81 + wb) * 256 + threadIdx.x;
      l = l < N3 ? l : 0;
      const u16* p = l < N1 ? pw1 + (size_t)l * 64 : (l < N2 ? pw2 + (size_t)(l - N1) * 64 : pw3 + (size_t)(l - N2) * 64);
      vv[q] = *(const unsigned*)p;
    }
    if (vv[0] + vv[1] + vv[2] + vv[3] == 0x9e3779b9u && threadIdx.x == 999) out[0] = 0;
    return;
  }
  const int lane = threadIdx.x & 63, row = blockIdx.x * 4 + (threadIdx.x >> 6);
  const int b = row >> 10;
  const u16* xr = x + (size_t)row * DM;
  float v[12];
#pragma unroll
  for (int i = 0; i < 3; ++i) {
    u32x2 u = *(const u32x2*)(xr + i * 256 + lane * 4);
    v[i * 4 + 0] = bflo(u[0]); v[i * 4 + 1] = bfhi(u[0]);
    v[i * 4 + 2] = bflo(u[1]); v[i * 4 + 3] = bfhi(u[1]);
  }
  const float* S = tab + (size_t)(b * 2 + which) * 2 * DM;
  const float* Tt = S + DM;
  f32x4 sc[3], sh[3];
#pragma unroll
  for (int i = 0; i < 3; ++i) {
    sc[i] = *(const f32x4*)(S + i * 256 + lane * 4);
    sh[i] = *(const f32x4*)(Tt + i * 256 + lane * 4);
  }
  float s = 0.f;
#pragma unroll
  for (int i = 0; i < 12; ++i) s += v[i];
#pragma unroll
  for (int o = 32; o; o >>= 1) s += __shfl_xor(s, o);
  const float mean = s * (1.f / DM);
  float q = 0.f;
#pragma unroll
  for (int i = 0; i < 12; ++i) { float d = v[i] - mean; q += d * d; }
#pragma unroll
  for (int o = 32; o; o >>= 1) q += __shfl_xor(q, o);
  const float mb = rbf(mean), vb = rbf(q * (1.f / DM));
  const float sd = rbf(__fsqrt_rn(rbf(vb + 1e-5f)));
#pragma unroll
  for (int i = 0; i < 3; ++i) {
    float h[4];
#pragma unroll
    for (int j = 0; j < 4; ++j) {
      float n = rbf(rbf(v[i * 4 + j] - mb) / sd);
      h[j] = rbf(n * sc[i][j]) + sh[i][j];
    }
    u32x2 o = {pack2(h[0], h[1]), pack2(h[2], h[3])};
    *(u32x2*)(out + (size_t)row * DM + i * 256 + lane * 4) = o;
  }
}

using GQKV = GC<128, 144, 4, 2, 4, true, false>;
__global__ __launch_bounds__(512, 2) void k_qkv(const u16* __restrict__ h, const u16* __restrict__ w,
                                                u16* __restrict__ qkv) {
  extern __shared__ __attribute__((aligned(16))) char smem[];
  using C = GQKV;
  constexpr int BM = 128, BN = 144;
  const int xc = blockIdx.x & 7, j = blockIdx.x >> 3;
  const int tm = (xc >> 2) * 8 + (j >> 2), tn = (xc & 3) * 4 + (j & 3);
  const int m0 = tm * BM, n0 = tn * BN;
  f32x4 acc[C::MT][C::NT] = {};
  gemm_main<BM, BN, 4, 2, 4, true, 12, 0>(h + (size_t)m0 * DM, DM, w + n0, 3 * DM, smem, acc, NoHook(), NoHook());
  const int lane = threadIdx.x & 63, wave = threadIdx.x >> 6, wm = wave % 4, wn = wave / 4;
#pragma unroll
  for (int m = 0; m < C::MT; ++m) {
    const int row = m0 + wm * C::WTM + m * 16 + (lane & 15);
    u16* rp = qkv + (size_t)row * (3 * DM) + n0 + wn * C::WTN;
    u32x2 v[C::NT];
#pragma unroll
    for (int n = 0; n < C::NT; ++n)
      v[n] = u32x2{pack2(acc[m][n][0], acc[m][n][1]), pack2(acc[m][n][2], acc[m][n][3])};
    store_pair(rp, v[0], v[1]);
    store_pair(rp + 32, v[2], v[3]);
    if (wn == 0) *(u32x2*)(rp + 64 + (lane >> 4) * 4) = v[4];
  }
}

using GSC = GC<128, 128, 4, 2, 4, false, false>;
__global__ __launch_bounds__(512, 2) void k_scores(const u16* __restrict__ qkv, u16* __restrict__ E,
                                                   f32x2* __restrict__ st) {
  extern __shared__ __attribute__((aligned(16))) char smem[];
  using C = GSC;
  constexpr int BM = 128, BN = 128;
  const int xc = blockIdx.x & 7, j = blockIdx.x >> 3;
  const int bh = xc >> 1, b = bh >> 1, hh = bh & 1;
  const int tm = (xc & 1) * 4 + (j >> 3), tn = j & 7;
  const int m0 = tm * BM, n0 = tn * BN;
  const u16* Q = qkv + (size_t)(b * NSEQ + m0) * (3 * DM) + hh * DH;
  const u16* K = qkv + (size_t)(b * NSEQ + n0) * (3 * DM) + DM + hh * DH;
  f32x4 acc[C::MT][C::NT] = {};
  gemm_main<BM, BN, 4, 2, 4, false, 6, 1>(Q, 3 * DM, K, 3 * DM, smem, acc, NoHook(), NoHook());
  const int lane = threadIdx.x & 63, wave = threadIdx.x >> 6, wm = wave % 4, wn = wave / 4;
  const float scale = 0.05103103630798288f;
#pragma unroll
  for (int m = 0; m < C::MT; ++m) {
    float s[C::NT][4];
    float mx = -INFINITY;
#pragma unroll
    for (int n = 0; n < C::NT; ++n) {
      unsigned p0 = pack2(acc[m][n][0], acc[m][n][1]), p1 = pack2(acc[m][n][2], acc[m][n][3]);
      p0 = pack2(bflo(p0) * scale, bfhi(p0) * scale);
      p1 = pack2(bflo(p1) * scale, bfhi(p1) * scale);
      s[n][0] = bflo(p0); s[n][1] = bfhi(p0); s[n][2] = bflo(p1); s[n][3] = bfhi(p1);
      mx = fmaxf(mx, fmaxf(fmaxf(s[n][0], s[n][1]), fmaxf(s[n][2], s[n][3])));
    }
    mx = fmaxf(mx, __shfl_xor(mx, 16));
    mx = fmaxf(mx, __shfl_xor(mx, 32));
    float sum = 0.f;
    const int row = m0 + wm * C::WTM + m * 16 + (lane & 15);
    u16* Er = E + ((size_t)bh * NSEQ + row) * NSEQ + n0 + wn * C::WTN;
    u32x2 v[C::NT];
#pragma unroll
    for (int n = 0; n < C::NT; ++n) {
#pragma unroll
      for (int e = 0; e < 4; ++e) {
        s[n][e] = __expf(s[n][e] - mx);
        sum += s[n][e];
      }
      v[n] = u32x2{pack2(s[n][0], s[n][1]), pack2(s[n][2], s[n][3])};
    }
    store_pair(Er, v[0], v[1]);
    store_pair(Er + 32, v[2], v[3]);
    sum += __shfl_xor(sum, 16);
    sum += __shfl_xor(sum, 32);
    if (lane < 16) st[((size_t)bh * NSEQ + row) * 16 + tn * 2 + wn] = f32x2{mx, sum};
  }
}

using GPV = GC<64, 96, 4, 2, 6, true, false>;
__global__ __launch_bounds__(512, 2) void k_pv(const u16* __restrict__ E, const f32x2* __restrict__ st,
                                               const u16* __restrict__ qkv, u16* __restrict__ O) {
  extern __shared__ __attribute__((aligned(16))) char smem[];
  using C = GPV;
  constexpr int BM = 64, BN = 96;
  const int xc = blockIdx.x & 7, j = blockIdx.x >> 3;
  const int bh = xc >> 1, b = bh >> 1, hh = bh & 1;
  const int tm = (xc & 1) * 8 + (j >> 2), tn = j & 3;
  const int m0 = tm * BM, n0 = tn * BN;
  const int lane = threadIdx.x & 63, wave = threadIdx.x >> 6, wm = wave % 4, wn = wave / 4;
  float* ftab = (float*)(smem + C::LDS);
  auto pre = [&]() {
    if (threadIdx.x < 64) {
      const f32x4* p = (const f32x4*)(st + ((size_t)bh * NSEQ + m0 + threadIdx.x) * 16);
      f32x4 v[8];
#pragma unroll
      for (int t = 0; t < 8; ++t) v[t] = p[t];
      float M = -INFINITY;
#pragma unroll
      for (int t = 0; t < 8; ++t) M = fmaxf(M, fmaxf(v[t][0], v[t][2]));
      float fac[16], Ls = 0.f;
#pragma unroll
      for (int t = 0; t < 8; ++t) {
        fac[2 * t] = __expf(v[t][0] - M);
        fac[2 * t + 1] = __expf(v[t][2] - M);
        Ls += fac[2 * t] * v[t][1] + fac[2 * t + 1] * v[t][3];
      }
      float inv = 1.f / Ls;
#pragma unroll
      for (int t = 0; t < 16; ++t) ftab[t * 64 + threadIdx.x] = fac[t] * inv;
      asm volatile("s_waitcnt lgkmcnt(0)" ::: "memory");
    }
  };
  const u16* A = E + ((size_t)bh * NSEQ + m0) * NSEQ;
  const u16* V = qkv + (size_t)(b * NSEQ) * (3 * DM) + 2 * DM + hh * DH + n0;
  f32x4 acc[1][3] = {};
  f32x4 tot[1][3] = {};
  const float* frow = ftab + wm * 16 + (lane & 15);
  auto hook = [&](int t) {
    const float f = frow[t * 64];
#pragma unroll
    for (int n = 0; n < 3; ++n) {
      tot[0][n] += acc[0][n] * f;
      acc[0][n] = f32x4{0.f, 0.f, 0.f, 0.f};
    }
  };
  gemm_main<BM, BN, 4, 2, 6, true, 16, 2>(A, NSEQ, V, 3 * DM, smem, acc, pre, hook);
#pragma unroll
  for (int n = 0; n < 3; ++n) {
    int row = b * NSEQ + m0 + wm * 16 + (lane & 15);
    int col = hh * DH + n0 + wn * C::WTN + n * 16 + (lane >> 4) * 4;
    u32x2 v = {pack2(tot[0][n][0], tot[0][n][1]), pack2(tot[0][n][2], tot[0][n][3])};
    *(u32x2*)(O + (size_t)row * DM + col) = v;
  }
}

template <int KI, int LDA, bool STATS>
__device__ __forceinline__ void res_gemm(const u16* __restrict__ A, const u16* __restrict__ W,
                                         const u16* __restrict__ xin, const float* __restrict__ gates,
                                         u16* __restrict__ xo, f32x2* __restrict__ pst, char* smem) {
  using C = GPV;
  constexpr int BM = 64, BN = 96;
  const int xc = blockIdx.x & 7, j = blockIdx.x >> 3;
  const int tm = (xc >> 1) * 8 + (j >> 2), tn = (xc & 1) * 4 + (j & 3);
  const int m0 = tm * BM, n0 = tn * BN, b = m0 >> 10;
  const int lane = threadIdx.x & 63, wave = threadIdx.x >> 6, wm = wave % 4, wn = wave / 4;
  const int row = m0 + wm * 16 + (lane & 15);
  const int col0 = n0 + wn * C::WTN + (lane >> 4) * 4;
  f32x4 acc[1][3] = {};
  gemm_main<BM, BN, 4, 2, 6, true, LDA / 64, KI>(A + (size_t)m0 * LDA, LDA, W + n0, DM, smem, acc, NoHook(), NoHook());
  float vals[12];
#pragma unroll
  for (int n = 0; n < 3; ++n) {
    u32x2 xv = *(const u32x2*)(xin + (size_t)row * DM + col0 + n * 16);
    f32x4 gt = *(const f32x4*)(gates + b * (2 * DM) + col0 + n * 16);
    unsigned a0 = pack2(acc[0][n][0], acc[0][n][1]), a1 = pack2(acc[0][n][2], acc[0][n][3]);
    unsigned g0 = pack2(gt[0] * bflo(a0), gt[1] * bfhi(a0));
    unsigned g1 = pack2(gt[2] * bflo(a1), gt[3] * bfhi(a1));
    u32x2 o = {pack2(bflo(xv[0]) + bflo(g0), bfhi(xv[0]) + bfhi(g0)),
               pack2(bflo(xv[1]) + bflo(g1), bfhi(xv[1]) + bfhi(g1))};
    *(u32x2*)(xo + (size_t)row * DM + col0 + n * 16) = o;
    vals[n * 4 + 0] = bflo(o[0]); vals[n * 4 + 1] = bfhi(o[0]);
    vals[n * 4 + 2] = bflo(o[1]); vals[n * 4 + 3] = bfhi(o[1]);
  }
  if (STATS) {
    float s = 0.f;
#pragma unroll
    for (int i = 0; i < 12; ++i) s += vals[i];
    s += __shfl_xor(s, 16);
    s += __shfl_xor(s, 32);
    const float lm = s * (1.f / 48.f);
    float q = 0.f;
#pragma unroll
    for (int i = 0; i < 12; ++i) { float d = vals[i] - lm; q += d * d; }
    q += __shfl_xor(q, 16);
    q += __shfl_xor(q, 32);
    if (lane < 16) pst[(size_t)row * 16 + tn * 2 + wn] = f32x2{s, q};
  }
}
__global__ __launch_bounds__(512, 2) void k_wo(const u16* __restrict__ O, const u16* __restrict__ wo,
                                               const u16* __restrict__ xin, const float* __restrict__ gates,
                                               u16* __restrict__ x1, f32x2* __restrict__ pst) {
  extern __shared__ __attribute__((aligned(16))) char smem[];
  res_gemm<3, DM, false>(O, wo, xin, gates, x1, pst, smem);
}
__global__ __launch_bounds__(512, 2) void k_fc2(const u16* __restrict__ f, const u16* __restrict__ w2,
                                                const u16* __restrict__ x1, const float* __restrict__ gates,
                                                u16* __restrict__ out) {
  extern __shared__ __attribute__((aligned(16))) char smem[];
  res_gemm<5, FF, false>(f, w2, x1, gates + DM, out, nullptr, smem);
}

__device__ __forceinline__ float gelu_tanh(float u) {
  float w = u * (-2.f * 0.7978845608028654f * 1.4426950408889634f);
  float a = w * (1.f + 0.044715f * u * u);
  return u * __builtin_amdgcn_rcpf(1.f + __builtin_amdgcn_exp2f(a));
}
using GFC = GC<128, 192, 2, 4, 4, true, false>;
__global__ __launch_bounds__(512, 2) void k_fc1(const u16* __restrict__ h2, const u16* __restrict__ w1,
                                                u16* __restrict__ f) {
  extern __shared__ __attribute__((aligned(16))) char smem[];
  using C = GFC;
  constexpr int BM = 128, BN = 192;
  const int xc = blockIdx.x & 7, j = blockIdx.x >> 3;
  const int tm = (xc >> 2) * 8 + (j >> 2), tn = (xc & 3) * 4 + (j & 3);
  const int m0 = tm * BM, n0 = tn * BN;
  f32x4 acc[C::MT][C::NT] = {};
  gemm_main<BM, BN, 2, 4, 4, true, 12, 4>(h2 + (size_t)m0 * DM, DM, w1 + n0, FF, smem, acc, NoHook(), NoHook());
  const int lane = threadIdx.x & 63, wave = threadIdx.x >> 6, wm = wave % 2, wn = wave / 2;
#pragma unroll
  for (int m = 0; m < C::MT; ++m) {
    const int row = m0 + wm * C::WTM + m * 16 + (lane & 15);
    u16* rp = f + (size_t)row * FF + n0 + wn * C::WTN;
    u32x2 v[C::NT];
#pragma unroll
    for (int n = 0; n < C::NT; ++n) {
      unsigned p0 = pack2(acc[m][n][0], acc[m][n][1]), p1 = pack2(acc[m][n][2], acc[m][n][3]);
      v[n] = u32x2{pack2(gelu_tanh(bflo(p0)), gelu_tanh(bfhi(p0))), pack2(gelu_tanh(bflo(p1)), gelu_tanh(bfhi(p1)))};
    }
    store_pair(rp, v[0], v[1]);
    *(u32x2*)(rp + 32 + (lane >> 4) * 4) = v[2];
  }
}

extern "C" void kernel_launch(void* const* d_in, const int* in_sizes, int n_in, void* d_out, int out_size,
                              void* d_ws, size_t ws_size, hipStream_t stream) {
  const u16* x = (const u16*)d_in[0];
  const u16* c = (const u16*)d_in[1];
  const u16* wqkv = (const u16*)d_in[2];
  const u16* wo = (const u16*)d_in[3];
  const u16* wmod = (const u16*)d_in[4];
  const u16* w1 = (const u16*)d_in[5];
  const u16* w2 = (const u16*)d_in[6];
  u16* out = (u16*)d_out;

  char* ws = (char*)d_ws;
  size_t o = 0;
  auto take = [&](size_t bytes) { char* p = ws + o; o += (bytes + 255) & ~(size_t)255; return p; };
  float* gates = (float*)take(2 * 2 * DM * 4);
  float* tab = (float*)take(2 * 2 * 2 * DM * 4);
  u16* h = (u16*)take((size_t)MROWS * DM * 2);
  u16* h2 = (u16*)take((size_t)MROWS * DM * 2);
  u16* qkv = (u16*)take((size_t)MROWS * 3 * DM * 2);
  u16* E = (u16*)take((size_t)4 * NSEQ * NSEQ * 2);
  f32x2* st = (f32x2*)take((size_t)4 * NSEQ * 16 * 8);
  u16* O = (u16*)take((size_t)MROWS * DM * 2);
  u16* x1 = (u16*)take((size_t)MROWS * DM * 2);
  f32x2* pst = (f32x2*)take((size_t)MROWS * 16 * 8);
  u16* f = (u16*)take((size_t)MROWS * FF * 2);

  constexpr int LDS_QKV = GQKV::LDS;
  constexpr int LDS_SC = GSC::LDS;
  constexpr int LDS_PV = GPV::LDS + 16 * 64 * 4;
  constexpr int LDS_WO = GPV::LDS;
  constexpr int LDS_FC = GFC::LDS;
  (void)hipFuncSetAttribute((const void*)k_qkv, hipFuncAttributeMaxDynamicSharedMemorySize, LDS_QKV);
  (void)hipFuncSetAttribute((const void*)k_scores, hipFuncAttributeMaxDynamicSharedMemorySize, LDS_SC);
  (void)hipFuncSetAttribute((const void*)k_pv, hipFuncAttributeMaxDynamicSharedMemorySize, LDS_PV);
  (void)hipFuncSetAttribute((const void*)k_wo, hipFuncAttributeMaxDynamicSharedMemorySize, LDS_WO);
  (void)hipFuncSetAttribute((const void*)k_fc1, hipFuncAttributeMaxDynamicSharedMemorySize, LDS_FC);
  (void)hipFuncSetAttribute((const void*)k_fc2, hipFuncAttributeMaxDynamicSharedMemorySize, LDS_WO);

  k_pre<<<144 + 14, 512, 0, stream>>>(c, wmod, gates, tab, wqkv);
  k_lnmod<<<MROWS / 4 + 81, 256, 0, stream>>>(x, tab, 0, h, wo, w1, w2);
  k_qkv<<<256, 512, LDS_QKV, stream>>>(h, wqkv, qkv);
  k_scores<<<256, 512, LDS_SC, stream>>>(qkv, E, st);
  k_pv<<<256, 512, LDS_PV, stream>>>(E, st, qkv, O);
  k_wo<<<256, 512, LDS_WO, stream>>>(O, wo, x, gates, x1, pst);
  k_lnmod<<<MROWS / 4, 256, 0, stream>>>(x1, tab, 1, h2, wo, w1, w2);
  k_fc1<<<256, 512, LDS_FC, stream>>>(h2, w1, f);
  k_fc2<<<256, 512, LDS_WO, stream>>>(f, w2, x1, gates, out);
}
```
